```python
import torch

def _softmax(x, axis=-1):
    return torch.softmax(x, dim=axis)
def _gelu(x):
    return torch.nn.functional.gelu(x, approximate="tanh")
def _silu(x):
    return torch.nn.functional.silu(x)
def _sigmoid(x):
    return torch.sigmoid(x)
def _layernorm(x, axis=-1, eps=1e-5):
    m = x.mean(dim=axis, keepdim=True)
    v = x.var(dim=axis, keepdim=True, unbiased=False)
    return (x - m) / torch.sqrt(v + eps)
def _scatter_add(tgt, idx, src, axis=0):
    return tgt.clone().index_add_(axis, idx, src)
def _segment_sum(src, seg, n):
    out = torch.zeros((n,) + src.shape[1:], dtype=src.dtype)
    return out.index_add_(0, seg, src)
def _pad(x, pw):
    flat = tuple(v for pair in reversed(pw) for v in pair)
    return torch.nn.functional.pad(x, flat)

kernel_name = 'recurrent_scan__ssm_conv_s4d__geglu_ffn__prompt'

def setup_inputs():

    _w = (lambda a: (lambda t: t.to(torch.bfloat16) if t.is_floating_point() else t)(torch.as_tensor(a)))
    def _A_setup():
        Bsz, L, D, N = 1, 2048, 640, 128
        return {
            "u": _w((lambda *s: torch.randn(*s, dtype=torch.bfloat16) * 0.05)(Bsz, L, D)),
            "log_A_real": _w((lambda *s: torch.randn(*s, dtype=torch.bfloat16) * 3.2)(D, N) * 0.5),
            "A_imag": _w((lambda *s: torch.randn(*s, dtype=torch.bfloat16) * 0.05)(D, N) * 3.14),
            "C_real": _w((lambda *s: torch.randn(*s, dtype=torch.bfloat16) * 0.1)(D, N) / N ** 0.5),
            "C_imag": _w((lambda *s: torch.randn(*s, dtype=torch.bfloat16) * 0.1)(D, N) / N ** 0.5),
            "log_dt": _w((lambda *s: torch.randn(*s, dtype=torch.bfloat16) * 0.05)(D) * 0.5 - 4.0),
            "D_skip": _w((lambda *s: torch.randn(*s, dtype=torch.bfloat16) * 0.05)(D)),
        }
    _A_in = _A_setup()

    def _B_setup():
        B, S, D, F = 1, 2048, 640, 1728
        return {
            "x": _w((lambda *s: torch.randn(*s, dtype=torch.bfloat16) * 0.05)(B, S, D)),
            "w_gate": _w((lambda *s: torch.randn(*s, dtype=torch.bfloat16) * 0.05)(D, F)),
            "w_up": _w((lambda *s: torch.randn(*s, dtype=torch.bfloat16) * 0.05)(D, F)),
            "w_down": _w((lambda *s: torch.randn(*s, dtype=torch.bfloat16) * 0.05)(F, D)),
        }
    _B_in = _B_setup()

    return {
        "A_u": _A_in["u"],
        "A_log_A_real": _A_in["log_A_real"],
        "A_A_imag": _A_in["A_imag"],
        "A_C_real": _A_in["C_real"],
        "A_C_imag": _A_in["C_imag"],
        "A_log_dt": _A_in["log_dt"],
        "A_D_skip": _A_in["D_skip"],
        "B_x": _B_in["x"],
        "B_w_gate": _B_in["w_gate"],
        "B_w_up": _B_in["w_up"],
        "B_w_down": _B_in["w_down"],
    }

def reference(A_u, A_log_A_real, A_A_imag, A_C_real, A_C_imag, A_log_dt, A_D_skip, B_x, B_w_gate, B_w_up, B_w_down):
    def _A_ref(u, log_A_real, A_imag, C_real, C_imag, log_dt, D_skip):
        L = u.shape[1]
        dt = torch.exp(log_dt)
        A = -torch.exp(log_A_real) + 1j * A_imag
        C = (C_real + 1j * C_imag) * dt[:, None]
        pos = (lambda x, dt: x.to(dt))(torch.arange(L), log_dt.dtype)
        P = torch.exp((A * dt[:, None])[:, :, None] * pos[None, None, :])
        K = 2.0 * torch.einsum("dn,dnl->dl", C, P).real
        L2 = 2 * L
        u_t = (lambda x, *d: x.permute(*d))(u, 0, 2, 1)
        Kf = (lambda x, *a, **k: torch.fft.rfft(x.float(), *a, **k))(K, L2)
        Uf = (lambda x, *a, **k: torch.fft.rfft(x.float(), *a, **k))(u_t, L2)
        y = (lambda x, *a, **k: torch.fft.irfft(x, *a, **k).to(torch.bfloat16))(Uf * Kf[None, :, :], L2)[:, :, :L]
        y = (lambda x, *d: x.permute(*d))(y, 0, 2, 1)
        return y + u * D_skip[None, None, :]

    def _B_ref(x, w_gate, w_up, w_down):
        return (_gelu(x @ w_gate) * (x @ w_up)) @ w_down

    x = A_u
    xn = _layernorm(x, axis=-1)
    s = _A_ref(xn, A_log_A_real, A_A_imag, A_C_real, A_C_imag, A_log_dt, A_D_skip)
    g = _silu(_B_ref(xn, B_w_gate, B_w_up, B_w_down))
    return x + s * g
```

```cpp
#include <hip/hip_runtime.h>
#include <hip/hip_bf16.h>
#include <stdint.h>

typedef unsigned short u16;
using bf16x8 = __attribute__((ext_vector_type(8))) short;
using f32x4 = __attribute__((ext_vector_type(4))) float;

constexpr int L = 2048, D = 640, N = 128, F = 1728;

__device__ __forceinline__ float bf2f(u16 v) { return __uint_as_float(((unsigned)v) << 16); }
__device__ __forceinline__ u16 f2bf(float f) { __hip_bfloat16 h = __float2bfloat16(f); return *reinterpret_cast<u16*>(&h); }
__device__ __forceinline__ float rbf(float f) { return bf2f(f2bf(f)); }

__global__ void ln_kernel(const u16* __restrict__ x, u16* __restrict__ xn, u16* __restrict__ xnT) {
  int row = blockIdx.x * 4 + (threadIdx.x >> 6);
  int lane = threadIdx.x & 63;
  float v[10];
  float s = 0;
  for (int i = 0; i < 10; i++) { v[i] = bf2f(x[row * D + lane + 64 * i]); s += v[i]; }
  for (int o = 32; o; o >>= 1) s += __shfl_xor(s, o);
  float mean = s / D;
  float q = 0;
  for (int i = 0; i < 10; i++) { float d = v[i] - mean; q += d * d; }
  for (int o = 32; o; o >>= 1) q += __shfl_xor(q, o);
  float var = rbf(q / D);
  float m = rbf(mean);
  float den = rbf(sqrtf(rbf(var + 1e-5f)));
  for (int i = 0; i < 10; i++) {
    u16 r = f2bf(rbf(v[i] - m) / den);
    xn[row * D + lane + 64 * i] = r;
    xnT[(lane + 64 * i) * L + row] = r;
  }
}

__global__ void kgen_kernel(const u16* lar, const u16* aim, const u16* cre, const u16* cim, const u16* ldt, float* K) {
  int d = blockIdx.x;
  __shared__ float sa[N], sb[N], sc[N], sd[N];
  float dt = rbf(expf(bf2f(ldt[d])));
  if (threadIdx.x < N) {
    int n = threadIdx.x;
    float ar = -rbf(expf(bf2f(lar[d * N + n])));
    float ai = bf2f(aim[d * N + n]);
    sa[n] = ar * dt; sb[n] = ai * dt;
    sc[n] = bf2f(cre[d * N + n]) * dt; sd[n] = bf2f(cim[d * N + n]) * dt;
  }
  __syncthreads();
  for (int l = threadIdx.x; l < L; l += blockDim.x) {
    float p = rbf((float)l);
    float acc = 0;
    for (int n = 0; n < N; n++) {
      float e = __expf(sa[n] * p);
      float s, c; __sincosf(sb[n] * p, &s, &c);
      acc += e * (sc[n] * c - sd[n] * s);
    }
    K[d * L + l] = 2.f * acc;
  }
}

__global__ void conv_kernel(const float* K, const u16* xnT, const u16* dsk, float* sT) {
  int d = blockIdx.x;
  __shared__ float sk[L], su[L];
  for (int l = threadIdx.x; l < L; l += blockDim.x) { sk[l] = K[d * L + l]; su[l] = bf2f(xnT[d * L + l]); }
  __syncthreads();
  float ds = bf2f(dsk[d]);
  for (int l = threadIdx.x; l < L; l += blockDim.x) {
    float acc = 0;
    for (int j = 0; j <= l; j++) acc += sk[j] * su[l - j];
    sT[d * L + l] = rbf(rbf(acc) + rbf(su[l] * ds));
  }
}

__global__ void gemm1_kernel(const u16* xn, const u16* wg, const u16* wu, u16* H) {
  int lane = threadIdx.x & 63, w = threadIdx.x >> 6;
  int m0 = blockIdx.y * 64 + w * 16, n0 = blockIdx.x * 16;
  f32x4 ag = {0, 0, 0, 0}, au = {0, 0, 0, 0};
  for (int k0 = 0; k0 < D; k0 += 32) {
    bf16x8 a = *(const bf16x8*)(xn + (m0 + (lane & 15)) * D + k0 + 8 * (lane >> 4));
    bf16x8 bg, bu;
    for (int j = 0; j < 8; j++) { bg[j] = wg[(k0 + 8 * (lane >> 4) + j) * F + n0 + (lane & 15)]; bu[j] = wu[(k0 + 8 * (lane >> 4) + j) * F + n0 + (lane & 15)]; }
    ag = __builtin_amdgcn_mfma_f32_16x16x32_bf16(a, bg, ag, 0, 0, 0);
    au = __builtin_amdgcn_mfma_f32_16x16x32_bf16(a, bu, au, 0, 0, 0);
  }
  for (int j = 0; j < 4; j++) {
    float g = rbf(ag[j]), u = rbf(au[j]);
    float t = 0.7978845608028654f * (g + 0.044715f * g * g * g);
    float ge = rbf(0.5f * g * (1.f + tanhf(t)));
    H[(m0 + (lane >> 4) * 4 + j) * F + n0 + (lane & 15)] = f2bf(ge * u);
  }
}

__global__ void gemm2_kernel(const u16* H, const u16* wd, const float* sT, const u16* x, u16* out) {
  int lane = threadIdx.x & 63, w = threadIdx.x >> 6;
  int m0 = blockIdx.y * 64 + w * 16, n0 = blockIdx.x * 16;
  f32x4 acc = {0, 0, 0, 0};
  for (int k0 = 0; k0 < F; k0 += 32) {
    bf16x8 a = *(const bf16x8*)(H + (m0 + (lane & 15)) * F + k0 + 8 * (lane >> 4));
    bf16x8 b;
    for (int j = 0; j < 8; j++) b[j] = wd[(k0 + 8 * (lane >> 4) + j) * D + n0 + (lane & 15)];
    acc = __builtin_amdgcn_mfma_f32_16x16x32_bf16(a, b, acc, 0, 0, 0);
  }
  for (int j = 0; j < 4; j++) {
    int r = m0 + (lane >> 4) * 4 + j, c = n0 + (lane & 15);
    float o = rbf(acc[j]);
    float g = rbf(o / (1.f + expf(-o)));
    float s = sT[c * L + r];
    out[r * D + c] = f2bf(bf2f(x[r * D + c]) + rbf(s * g));
  }
}

extern "C" void kernel_launch(void* const* d_in, const int* in_sizes, int n_in, void* d_out, int out_size, void* d_ws, size_t ws_size, hipStream_t stream) {
  const u16* x = (const u16*)d_in[0];
  const u16* lar = (const u16*)d_in[1];
  const u16* aim = (const u16*)d_in[2];
  const u16* cre = (const u16*)d_in[3];
  const u16* cim = (const u16*)d_in[4];
  const u16* ldt = (const u16*)d_in[5];
  const u16* dsk = (const u16*)d_in[6];
  const u16* wg = (const u16*)d_in[8];
  const u16* wu = (const u16*)d_in[9];
  const u16* wd = (const u16*)d_in[10];
  char* ws = (char*)d_ws;
  u16* xn = (u16*)ws; ws += L * D * 2;
  u16* xnT = (u16*)ws; ws += L * D * 2;
  float* K = (float*)ws; ws += L * D * 4;
  float* sT = (float*)ws; ws += L * D * 4;
  u16* H = (u16*)ws; ws += L * F * 2;
  ln_kernel<<<L / 4, 256, 0, stream>>>(x, xn, xnT);
  kgen_kernel<<<D, 256, 0, stream>>>(lar, aim, cre, cim, ldt, K);
  conv_kernel<<<D, 256, 0, stream>>>(K, xnT, dsk, sT);
  gemm1_kernel<<<dim3(F / 16, L / 64), 256, 0, stream>>>(xn, wg, wu, H);
  gemm2_kernel<<<dim3(D / 16, L / 64), 256, 0, stream>>>(H, wd, sT, x, (u16*)d_out);
}
```

```cpp
#include <hip/hip_runtime.h>
#include <stdint.h>

typedef unsigned short u16;
typedef unsigned long long u64;
typedef __attribute__((ext_vector_type(4))) short s16x4;
typedef __attribute__((ext_vector_type(8))) __bf16 bf16x8;
typedef __attribute__((ext_vector_type(2))) __bf16 bf16x2;
typedef __attribute__((ext_vector_type(2))) float f32x2;
typedef __attribute__((ext_vector_type(4))) float f32x4;
typedef __attribute__((ext_vector_type(16))) float f32x16;
typedef __attribute__((ext_vector_type(2))) unsigned u32x2;
typedef __attribute__((ext_vector_type(4))) unsigned u32x4;
#define LDS __attribute__((address_space(3)))
#define GAS __attribute__((address_space(1)))
#define DI __device__ __forceinline__
#define RLX __ATOMIC_RELAXED, __HIP_MEMORY_SCOPE_AGENT
#define SC1 16

constexpr int L = 2048, D = 640, F = 1728;
constexpr unsigned O_E = 0, O_FA = 1024, O_FK = 2048, O_FH = 8192  , O_FS = 16384;
constexpr unsigned O_STAT = 524288, O_XNB = 1u << 20, O_WDT = 4u << 20, O_KX = 7u << 20, O_HT = 13u << 20, O_ST = 21u << 20, WS_BYTES = 24u << 20;
constexpr unsigned KXB = 8448, KCB = 1536;
constexpr unsigned SLOT = 49152;
constexpr unsigned L_WD = 0, WDW = 25600;
constexpr unsigned L_CONV = 102400, CONVB = 13056;
constexpr unsigned L_S = 147456;
constexpr unsigned L_CS = 159744;
constexpr unsigned L_ST = 160256;
constexpr unsigned L_FULL = 161280, L_FREE = L_FULL + 16, L_RDY = L_FULL + 32, L_FLAG = L_FULL + 48, L_DUMP = L_FULL + 256;
constexpr unsigned LDS_BYTES = 163840;
constexpr unsigned AUXW = 20480;
#define SPL 20000
constexpr int SPIN_LIMIT = SPL;

#define ST(i) do {} while (0)
#define ST_INIT() do {} while (0)
#define ST_DUMP() do {} while (0)

DI float bf_lo(unsigned v) { return __uint_as_float(v << 16); }
DI float bf_hi(unsigned v) { return __uint_as_float(v & 0xffff0000u); }
DI unsigned pk2(float a, float b) { return __builtin_bit_cast(unsigned, __builtin_convertvector(f32x2{a, b}, bf16x2)); }
DI float rbf(float a) { return bf_lo(pk2(a, a) & 0xffffu); }

DI void glds(unsigned lds, unsigned voff, const char* sbase) {
  asm volatile("s_mov_b32 m0, %0\n\ts_nop 0\n\tglobal_load_lds_dwordx4 %1, %2" :: "s"(lds), "v"(voff), "s"(sbase) : "memory");
}
DI void glds1(unsigned lds, unsigned voff, const char* sbase) {
  asm volatile("s_mov_b32 m0, %0\n\ts_nop 0\n\tglobal_load_lds_dwordx4 %1, %2 sc1" :: "s"(lds), "v"(voff), "s"(sbase) : "memory");
}
DI void touch(unsigned lds, const char* p) { asm volatile("s_mov_b32 m0, %0\n\ts_nop 0\n\tglobal_load_lds_dword %1, off" :: "s"(lds), "v"(p) : "memory"); }
DI void touch1(unsigned lds, unsigned voff, const char* sbase) { asm volatile("s_mov_b32 m0, %0\n\ts_nop 0\n\tglobal_load_lds_dword %1, %2 sc1" :: "s"(lds), "v"(voff), "s"(sbase) : "memory"); }
template<int N> DI void wait_vm() { asm volatile("s_waitcnt vmcnt(%0)" :: "i"(N) : "memory"); }
template<int B> DI void wait_vmx(int n) {
  if (n == 0) wait_vm<B>();
  else if (n < 3) { if (n == 1) wait_vm<B + 1>(); else wait_vm<B + 2>(); }
  else if (n < 5) { if (n == 3) wait_vm<B + 3>(); else wait_vm<B + 4>(); }
  else if (n == 5) wait_vm<B + 5>(); else wait_vm<B + 6>();
}
DI void sb() { __builtin_amdgcn_sched_barrier(0); }
DI void bar() { sb(); __builtin_amdgcn_s_barrier(); sb(); }
DI void wait_lgkm0() { __builtin_amdgcn_s_waitcnt(0xC07F); }
DI void wait_lgkm0m() { asm volatile("s_waitcnt lgkmcnt(0)" ::: "memory"); }
DI s16x4 trd(unsigned a) { return __builtin_amdgcn_ds_read_tr16_b64_v4i16((LDS s16x4*)(size_t)a); }
DI bf16x8 tr2(unsigned a, unsigned b) { return __builtin_bit_cast(bf16x8, __builtin_shufflevector(trd(a), trd(b), 0, 1, 2, 3, 4, 5, 6, 7)); }
DI bf16x8 ld128(unsigned a) { return *(LDS bf16x8*)(size_t)a; }
#define MFMA32(a, b, c) __builtin_amdgcn_mfma_f32_32x32x16_bf16(a, b, c, 0, 0, 0)
#define MFMA16(a, b, c) __builtin_amdgcn_mfma_f32_16x16x32_bf16(a, b, c, 0, 0, 0)
#define SGB(m, n) __builtin_amdgcn_sched_group_barrier(m, n, 0)
typedef __amdgpu_buffer_rsrc_t rsrc_t;
DI void mf16(f32x4& c, const bf16x8& a, const bf16x8& b) { asm volatile("v_mfma_f32_16x16x32_bf16 %0, %1, %2, %0" : "+v"(c) : "v"(a), "v"(b)); }
DI void mf16z(f32x4& c, const bf16x8& a, const bf16x8& b) { asm volatile("v_mfma_f32_16x16x32_bf16 %0, %1, %2, 0" : "=&v"(c) : "v"(a), "v"(b)); }
DI void mf_pad() { asm volatile("s_nop 7\n\ts_nop 1" ::: "memory"); }
typedef u32x2 __attribute__((aligned(4))) u32x2a;

DI void poll(const char* ws, unsigned off, unsigned off2, u64 tag) {
  #pragma unroll 1
  for (int it = 0; it < SPIN_LIMIT; it++) {
    const u64 v = __hip_atomic_load((const GAS u64*)(ws + off), RLX), v2 = __hip_atomic_load((const GAS u64*)(ws + off2), RLX);
    if (__all(v == tag && v2 == tag)) break;
    __builtin_amdgcn_s_sleep(2);
  }
  asm volatile("" ::: "memory");
}
DI void poll(const char* ws, unsigned off, u64 tag) {
  #pragma unroll 1
  for (int it = 0; it < SPIN_LIMIT; it++) {
    if (__all(__hip_atomic_load((const GAS u64*)(ws + off), RLX) == tag)) break;
    __builtin_amdgcn_s_sleep(2);
  }
  asm volatile("" ::: "memory");
}
DI void publish(char* ws, unsigned off, u64 tag, int lane) {
  asm volatile("s_waitcnt vmcnt(0)" ::: "memory");
  if (lane == 0) __hip_atomic_store((GAS u64*)(ws + off), tag, RLX);
}
DI void lds_wait(unsigned a) {
  #pragma unroll 1
  for (int it = 0; it < 16 * SPIN_LIMIT; it++) {
    if (__builtin_amdgcn_readfirstlane(*(volatile LDS unsigned*)(size_t)a)) break;
    __builtin_amdgcn_s_sleep(1);
  }
  asm volatile("" ::: "memory");
}
DI unsigned min4(u32x4 v) { return __builtin_amdgcn_readfirstlane(min(min(v[0], v[1]), min(v[2], v[3]))); }
DI unsigned lds_min4(unsigned a) { return min4(*(volatile LDS u32x4*)(size_t)a); }
template<int SL> DI void lds_wait4(unsigned a, unsigned need) {
  #pragma unroll 1
  for (int it = 0; it < 16 * SPIN_LIMIT && lds_min4(a) < need; it++) __builtin_amdgcn_s_sleep(SL);
  asm volatile("" ::: "memory");
}
DI void lds_put(unsigned a, unsigned v) { if ((threadIdx.x & 63) == 0) *(volatile LDS unsigned*)(size_t)a = v; }
DI int pos2ch(int pos) { return (pos & ~127) + 2 * (pos & 63) + ((pos >> 6) & 1); }

DI float red8(const float (&a)[8], int lane) {
  float b[4], c[2];
  #pragma unroll
  for (int i = 0; i < 4; i++) { auto r = __builtin_amdgcn_permlane32_swap(__float_as_uint(a[2 * i]), __float_as_uint(a[2 * i + 1]), false, false); b[i] = __uint_as_float(r[0]) + __uint_as_float(r[1]); }
  #pragma unroll
  for (int i = 0; i < 2; i++) { auto r = __builtin_amdgcn_permlane16_swap(__float_as_uint(b[2 * i]), __float_as_uint(b[2 * i + 1]), false, false); c[i] = __uint_as_float(r[0]) + __uint_as_float(r[1]); }
  bool up = lane & 8;
  float keep = up ? c[1] : c[0], send = up ? c[0] : c[1];
  float d = keep + __uint_as_float(__builtin_amdgcn_update_dpp(0, __float_as_uint(send), 0x128, 0xf, 0xf, true));
  d += __uint_as_float(__builtin_amdgcn_update_dpp(0, __float_as_uint(d), 0x141, 0xf, 0xf, true));
  d += __uint_as_float(__builtin_amdgcn_update_dpp(0, __float_as_uint(d), 0xB1, 0xf, 0xf, true));
  d += __uint_as_float(__builtin_amdgcn_update_dpp(0, __float_as_uint(d), 0x4E, 0xf, 0xf, true));
  return d;
}
DI constexpr int red8_lane(int i) { return ((i & 1) << 5) | (((i >> 1) & 1) << 4) | (((i >> 2) & 1) << 3); }
DI float rdl(float v, int l) { return __uint_as_float(__builtin_amdgcn_readlane(__float_as_uint(v), l)); }

DI f32x2 cmul(f32x2 a, f32x2 b) {
  f32x2 r;
  asm("v_pk_mul_f32 %0, %1, %2 op_sel:[1,1] op_sel_hi:[1,0] neg_lo:[0,1]\n\tv_pk_fma_f32 %0, %1, %2, %0 op_sel_hi:[0,1,1]" : "=&v"(r) : "v"(a), "v"(b));
  return r;
}
DI f32x2 zpow(float al, float be, float p) {
  float e = __builtin_amdgcn_exp2f(al * p), f = __builtin_amdgcn_fractf(be * p);
  return f32x2{e * __builtin_amdgcn_cosf(f), e * __builtin_amdgcn_sinf(f)};
}

typedef __attribute__((ext_vector_type(2))) _Float16 h2;
typedef __attribute__((ext_vector_type(8))) _Float16 f16x8;
DI h2 cmulh(h2 a, h2 b) { h2 t = h2{a[1], a[1]} * h2{-b[1], b[0]}; return __builtin_elementwise_fma(h2{a[0], a[0]}, b, t); }
DI h2 toh(f32x2 a) { return __builtin_convertvector(a, h2); }
DI h2 tohc(f32x2 a) { h2 r; asm("v_cvt_pk_f16_f32 %0, %1, -%2" : "=v"(r) : "v"(a[0]), "v"(a[1])); return r; }
template<int C> DI float maxd(float v) { return fmaxf(v, __uint_as_float(__builtin_amdgcn_update_dpp(0, __float_as_uint(v), C, 0xf, 0xf, true))); }

struct KPar { unsigned lar, aim, cre, cim; unsigned ldt; };
DI KPar kpar_load(const u16* lar, const u16* aim, const u16* cre, const u16* cim, const u16* ldt, int ch, int lane) {
  KPar p; int o = ch * 64 + lane;
  p.lar = ((const unsigned*)lar)[o]; p.aim = ((const unsigned*)aim)[o]; p.cre = ((const unsigned*)cre)[o]; p.cim = ((const unsigned*)cim)[o];
  p.ldt = ldt[ch];
  return p;
}
DI void kgen(const KPar& P, unsigned S, rsrc_t rw, unsigned goff, int lane) {
  const float LOG2E = 1.4426950408889634f;
  const float dt = rbf(__builtin_amdgcn_exp2f(bf_lo(P.ldt) * LOG2E));
  f32x2 C[2] = {f32x2{bf_lo(P.cre), bf_lo(P.cim)}, f32x2{bf_hi(P.cre), bf_hi(P.cim)}};
  float mx = fmaxf(fmaxf(fabsf(C[0][0]), fabsf(C[0][1])), fmaxf(fabsf(C[1][0]), fabsf(C[1][1])));
  mx = maxd<0xB1>(mx); mx = maxd<0x4E>(mx); mx = maxd<0x141>(mx); mx = maxd<0x140>(mx);
  mx = fmaxf(fmaxf(rdl(mx, 0), rdl(mx, 16)), fmaxf(rdl(mx, 32), rdl(mx, 48)));
  const int ex = __builtin_amdgcn_frexp_expf(mx);
  const float cs = __builtin_amdgcn_ldexpf(1.f, -ex), ks = __builtin_amdgcn_ldexpf(2.f * dt, ex);
  h2 T[2][8], Pw[2][4], Q[2][4], U[2][17];
  #pragma unroll
  for (int m = 0; m < 2; m++) {
    float la = m ? bf_hi(P.lar) : bf_lo(P.lar), ai = m ? bf_hi(P.aim) : bf_lo(P.aim);
    float al = -rbf(__builtin_amdgcn_exp2f(la * LOG2E)) * dt * LOG2E, be = ai * dt * 0.15915494309189535f;
    f32x2 z1 = zpow(al, be, 1.f), u1 = zpow(al, be, 128.f);
    f32x2 z2 = cmul(z1, z1), z3 = cmul(z2, z1), z4 = cmul(z2, z2), z8 = cmul(z4, z4), z16 = cmul(z8, z8), z24 = cmul(z16, z8), z32 = cmul(z16, z16);
    f32x2 q0 = C[m] * cs, q1 = cmul(q0, z32), q2 = cmul(q1, z32), q3 = cmul(q2, z32);
    f32x2 u2 = cmul(u1, u1), u3 = cmul(u2, u1), u4 = cmul(u2, u2), u8 = cmul(u4, u4), u12 = cmul(u8, u4), u16 = cmul(u8, u8);
    T[m][0] = h2{1, 0}; T[m][1] = toh(z1); T[m][2] = toh(z2); T[m][3] = toh(z3); T[m][4] = toh(z4);
    Pw[m][1] = tohc(z8); Pw[m][2] = tohc(z16); Pw[m][3] = tohc(z24);
    Q[m][0] = tohc(q0); Q[m][1] = tohc(q1); Q[m][2] = tohc(q2); Q[m][3] = tohc(q3);
    U[m][1] = toh(u1); U[m][2] = toh(u2); U[m][3] = toh(u3); U[m][4] = toh(u4); U[m][8] = toh(u8); U[m][12] = toh(u12); U[m][16] = toh(u16);
  }
  const unsigned wa = S + lane * 8, ra = S + (lane & 15) * 528 + (lane >> 4) * 16;
  auto put = [&](int row, h2 v0, h2 v1) { *(LDS u32x2*)((LDS char*)(size_t)wa + row * 528) = u32x2{__builtin_bit_cast(unsigned, v0), __builtin_bit_cast(unsigned, v1)}; };
  auto frag = [&](int k) { return *(LDS f16x8*)((LDS char*)(size_t)ra + k * 64); };
  f16x8 fine[8];
  #pragma unroll
  for (int b = 0; b < 16; b++) { if (b & 3) put(b, cmulh(Q[0][b >> 2], Pw[0][b & 3]), cmulh(Q[1][b >> 2], Pw[1][b & 3])); else put(b, Q[0][b >> 2], Q[1][b >> 2]); }
  #pragma unroll
  for (int k = 0; k < 8; k++) fine[k] = frag(k);
  f32x4 acc[3];
  auto tile = [&](int t) {
    f32x4 a = {0, 0, 0, 0}, a2 = {0, 0, 0, 0};
    #pragma unroll
    for (int k = 0; k < 8; k += 2) { a = __builtin_amdgcn_mfma_f32_16x16x32_f16(frag(k), fine[k], a, 0, 0, 0); a2 = __builtin_amdgcn_mfma_f32_16x16x32_f16(frag(k + 1), fine[k + 1], a2, 0, 0, 0); }
    acc[t] = (a + a2) * ks;
  };
  #pragma unroll
  for (int e = 5; e < 8; e++) { T[0][e] = cmulh(T[0][4], T[0][e - 4]); T[1][e] = cmulh(T[1][4], T[1][e - 4]); }
  #pragma unroll
  for (int e = 0; e < 8; e++) put(e, T[0][e], T[1][e]);
  put(8, U[0][1], U[1][1]);
  #pragma unroll
  for (int e = 1; e < 8; e++) put(8 + e, cmulh(T[0][e], U[0][1]), cmulh(T[1][e], U[1][1]));
  tile(0);
  #pragma unroll
  for (int a = 2; a < 4; a++) {
    put(4 * (a - 2), U[0][a], U[1][a]);
    #pragma unroll
    for (int e = 1; e < 4; e++) put(4 * (a - 2) + e, cmulh(T[0][2 * e], U[0][a]), cmulh(T[1][2 * e], U[1][a]));
  }
  #pragma unroll
  for (int a = 4; a < 8; a++) {
    if (a > 4) { U[0][a] = cmulh(U[0][4], U[0][a - 4]); U[1][a] = cmulh(U[1][4], U[1][a - 4]); }
    put(8 + 2 * (a - 4), U[0][a], U[1][a]);
    put(9 + 2 * (a - 4), cmulh(T[0][4], U[0][a]), cmulh(T[1][4], U[1][a]));
  }
  tile(1);
  #pragma unroll
  for (int a = 8; a <= 16; a++) { if (a & 3) put(a - 8, cmulh(U[0][a & ~3], U[0][a & 3]), cmulh(U[1][a & ~3], U[1][a & 3])); else put(a - 8, U[0][a], U[1][a]); }
  tile(2);
  __builtin_amdgcn_raw_buffer_store_b128(u32x4{pk2(acc[0][0], acc[0][1]), pk2(acc[0][2], acc[0][3]), pk2(acc[1][0], acc[1][1]), pk2(acc[1][2], acc[1][3])}, rw, goff + lane * 16, 0, SC1);
  __builtin_amdgcn_raw_buffer_store_b64(u32x2{pk2(acc[2][0], acc[2][1]), pk2(acc[2][2], acc[2][3])}, rw, goff + 1024 + lane * 8, 0, SC1);
}
DI unsigned prm(unsigned hi, unsigned lo, unsigned sel) { return __builtin_amdgcn_perm(hi, lo, sel); }
DI void kexpand(u32x4 k01, u32x2 k2, unsigned S, int lane) {
  const int b = lane & 15, g = lane >> 4;
  const unsigned TR = S + 4608;
  const unsigned LL = 0x01000100u, HH = 0x03020302u;
  unsigned a3 = g < 2 ? S + 8 + 2 * (2044 - (1024 + 512 * g + 384 + 8 * b)) : TR;
  unsigned a4 = (g < 2 && !(b & 1)) ? a3 - 2 : TR;
  unsigned v3[4];
  #pragma unroll
  for (int j = 0; j < 4; j++) { v3[j] = prm(0, k2[j >> 1], (j & 1) ? HH : LL); *(LDS u32x4*)((LDS char*)(size_t)a3 + 256 * (3 - j)) = u32x4{v3[j], v3[j], v3[j], v3[j]}; }
  *(LDS u32x4*)(size_t)(lane == 32 ? S : TR) = u32x4{v3[0], v3[0], v3[0], v3[0]};
  #pragma unroll
  for (int j = 0; j < 4; j++) *(volatile LDS u16*)((LDS char*)(size_t)a4 + 256 * (3 - j)) = (u16)v3[j];
  unsigned a2 = g >= 2 ? S + 8 + 2 * (2042 - (512 + 256 * (g - 2) + 128 + 8 * b)) : TR;
  const unsigned l0_ = prm(0, k01[2], LL), h0_ = prm(0, k01[2], HH), l1_ = prm(0, k01[3], LL), h1_ = prm(0, k01[3], HH);
  #pragma unroll
  for (int h = 0; h < 2; h++) {
    volatile LDS unsigned* q = (volatile LDS unsigned*)((LDS char*)(size_t)a2 + 256 * (1 - h));
    q[0] = h ? h1_ : h0_; q[1] = prm(0, k01[2 + h], 0x01000302u); q[2] = h ? l1_ : l0_; q[3] = h ? l1_ : l0_;
  }
  LDS char* a1 = (LDS char*)(size_t)(g < 2 ? S + 8 + 2 * (2041 - (256 + 128 * g + 8 * b)) : TR + 2);
  *(volatile LDS u16*)a1 = (u16)h1_;
  *(volatile LDS unsigned*)(a1 + 2) = l1_;
  *(volatile LDS unsigned*)(a1 + 6) = prm(k01[2], k01[3], 0x07060100u);
  *(volatile LDS unsigned*)(a1 + 10) = l0_;
  *(volatile LDS u16*)(a1 + 14) = (u16)l0_;
  unsigned a0 = S + 8 + 2 * (2044 - (128 * (g >> 1) + 4 * (g & 1) + 8 * b));
  *(volatile LDS u32x2*)(size_t)a0 = u32x2{prm(0, k01[1], 0x01000302u), prm(0, k01[0], 0x01000302u)};
  *(volatile LDS u32x2*)(size_t)(lane < 6 ? S + 8 + 4096 + 8 * lane : TR) = u32x2{0, 0};
  asm volatile("" ::: "memory");
  u32x2 x[5], y[5]; unsigned z[5];
  #pragma unroll
  for (int i = 0; i < 5; i++) {
    LDS char* a = (LDS char*)(size_t)(S + 8 + 16 * (i < 4 ? lane : (lane & 1))) + 1024 * i;
    x[i] = *(LDS u32x2*)a; y[i] = *(LDS u32x2*)(a + 8); z[i] = *(LDS unsigned*)(a + 16);
  }
  asm volatile("" ::: "memory");
  #pragma unroll
  for (int i = 0; i < 5; i++) {
    LDS char* a = (LDS char*)(size_t)(S + 4168 + 16 * (i < 4 ? lane : (lane & 1))) + 1024 * i;
    if (i < 4 || lane < 2) {
      *(LDS u32x2*)a = u32x2{__builtin_amdgcn_alignbit(x[i][1], x[i][0], 16), __builtin_amdgcn_alignbit(y[i][0], x[i][1], 16)};
      *(LDS u32x2*)(a + 8) = u32x2{__builtin_amdgcn_alignbit(y[i][1], y[i][0], 16), __builtin_amdgcn_alignbit(z[i], y[i][1], 16)};
    }
  }
  asm volatile("" ::: "memory");
}

DI void conv(unsigned KB, unsigned UB, float dsk, rsrc_t rw, unsigned goff, int lane) {
  const int r = lane & 15, kg = lane >> 4;
  const int n0 = 2047 - (16 * 112 + 16 * (kg >> 1) + r - 8 * (kg & 1));
  unsigned ka = KB + 8 + ((n0 & 1) ? 4160 + 2 * (n0 - 1) : 2 * n0);
  unsigned ua = UB + 2 * (256 + 16 * r - 16 * (kg >> 1) + 8 * (kg & 1));
  f32x4 acc[8];
  #pragma unroll
  for (int t = 0; t < 8; t++) { acc[t] = f32x4{0, 0, 0, 0}; asm volatile("" : "+v"(acc[t])); }
  bf16x8 A[2][8], Fr[2][8];
  auto ldq = [&](int s, int q, unsigned k, unsigned u) {
    unsigned a = k + (7 - q) * 512;
    u32x2 lo = *(LDS u32x2a*)(size_t)a, hi = *(LDS u32x2a*)(size_t)(a + 8);
    A[s][q] = __builtin_bit_cast(bf16x8, u32x4{lo[0], lo[1], hi[0], hi[1]});
    Fr[s][q] = ld128(u + q * 512);
  };
  auto step = [&](int s, bool nxt, unsigned k, unsigned u) {
    int n = 0;
    #pragma unroll
    for (int d = 0; d < 8; d++)
      #pragma unroll
      for (int q = 0; q <= d; q++) {
        mf16(acc[d], A[s][q], Fr[s][d - q]);
        if ((n & 3) == 1 && (n >> 2) < 8 && nxt) ldq(s ^ 1, n >> 2, k, u);
        n++;
      }
  };
  #pragma unroll
  for (int q = 0; q < 8; q++) ldq(0, q, ka, ua);
  #pragma unroll 1
  for (int it = 0; it < 3; it++) {
    step(0, true, ka - 64, ua - 64);
    ka -= 128; ua -= 128;
    step(1, true, ka, ua);
  }
  step(0, true, ka - 64, ua - 64);
  step(1, false, 0, 0);
  mf_pad();
  const unsigned ue = UB + 2 * (256 + 16 * r + 4 * kg);
  #pragma unroll
  for (int t = 0; t < 8; t++) {
    u32x2 u = *(LDS u32x2*)(size_t)(ue + 512 * t);
    u32x2 o = {pk2(fmaf(bf_lo(u[0]), dsk, acc[t][0]), fmaf(bf_hi(u[0]), dsk, acc[t][1])), pk2(fmaf(bf_lo(u[1]), dsk, acc[t][2]), fmaf(bf_hi(u[1]), dsk, acc[t][3]))};
    __builtin_amdgcn_raw_buffer_store_b64(o, rw, goff + 512 * t + 32 * r + 8 * kg, 0, SC1);
  }
}

__global__ __launch_bounds__(512, 2) void fused_kernel(const u16* x, const u16* __restrict__ lar, const u16* __restrict__ aim, const u16* __restrict__ cre,
    const u16* __restrict__ cim, const u16* __restrict__ ldt, const u16* dsk, const u16* wg, const u16* wu, const u16* wd,
    u16* out, char* ws) {
  extern __shared__ __attribute__((aligned(16))) char smem[];
  const int b = blockIdx.x, xcd = b & 7, r = b >> 3;
  const int w = __builtin_amdgcn_readfirstlane(threadIdx.x >> 6), lane = threadIdx.x & 63;
  const unsigned l0 = (unsigned)(size_t)(LDS char*)smem;
  asm volatile("" : "+s"(x), "+s"(wg), "+s"(wu), "+s"(wd), "+s"(ws), "+s"(out), "+s"(dsk));
  const rsrc_t rw = __builtin_amdgcn_make_buffer_rsrc(ws, 0, WS_BYTES, 0x00020000);
  ST_INIT(); ST(0);
  const bool gemm = r < 27;
  const int gi = xcd * 27 + r;
  const int id4 = (xcd & 3) * 27 + r, slice = id4 >> 2, panel = (xcd >> 2) * 4 + (id4 & 3);
#define TL 0
#define G2MAP 0
  const int rt = G2MAP ? (xcd >> 1) * 8 + (r >> 2) : xcd * 4 + (r >> 3), cs = G2MAP ? (xcd & 1) * 4 + (r & 3) : r & 7;
  u32x2 e0;
  asm volatile("global_load_dwordx2 %0, %1, %2 sc1" : "=&v"(e0) : "v"(0u), "s"(ws + O_E) : "memory");
  #define GET_TAG(N) do { unsigned lo_, hi_; asm volatile("s_waitcnt vmcnt(" #N ")\n\tv_readfirstlane_b32 %0, %2\n\tv_readfirstlane_b32 %1, %3" : "=&s"(lo_), "=&s"(hi_) : "v"(e0[0]), "v"(e0[1]) : "memory"); \
    tag = (((u64)hi_ << 32) | lo_) * 6364136223846793005ull + 1442695040888963407ull; } while (0)
  u64 tag;

  const int rot = G2MAP ? ((rt & 7) * 7 + (cs & 3) * 14) >> 2 : (cs * 7 + (rt & 3) * 14) >> 2;
  auto kidx = [&](int wv, int i) { int n = wv < 2 ? 14 : 13, v = i + rot; v -= v >= n ? n : 0; v -= v >= n ? n : 0; return wv + 4 * v; };
  auto wd_to_lds = [&](int i0, int i1) {
    #pragma unroll 1
    for (int i = i0; i < i1; i++) {
      const int cw = i / 5;
      const char* src = ws + O_WDT + (size_t)(kidx(cw, 4 + i - cw * 5) * 40 + cs * 5) * 1024;
      wait_vm<50>();
      #pragma unroll
      for (int c = 0; c < 5; c++) glds1(l0 + L_WD + i * 5120 + c * 1024, lane * 16, src + c * 1024);
    }
  };
  const int g = lane >> 4, q = (lane & 15) >> 2, p = lane & 3;

  if (gemm) {
    if (w >= 4) {
      const int j = w - 4, gu = j & 1;
      unsigned xo[2], wo[2];
      #pragma unroll
      for (int e = 0; e < 2; e++) { xo[e] = (lane >> 3) * 1280 + (((lane & 7) ^ ((4 * e + (lane >> 4)) & 7)) * 16); wo[e] = (lane >> 3) * 3456 + (((lane & 7) ^ (2 * (((lane >> 4) & 1) | (e << 1)))) * 16); }
      const char* xb = (const char*)x + (size_t)(panel * 256 + j * 64) * 1280;
      const char* wb = (const char*)(gu ? wu : wg) + (size_t)(32 * (j >> 1)) * 3456 + slice * 128;
      auto issueW = [&](int s, unsigned so) {
        #pragma unroll
        for (int i = 0; i < 4; i++) glds(l0 + so + 32768 + gu * 8192 + (j >> 1) * 4096 + i * 1024, wo[i & 1], wb + (size_t)(s * 64 + 8 * i) * 3456);
      };
      auto issueX = [&](int s, unsigned so) {
        #pragma unroll
        for (int i = 0; i < 8; i++) glds(l0 + so + j * 8192 + i * 1024, xo[i & 1], xb + i * 8 * 1280 + s * 128);
      };
      const unsigned ca = l0 + 32768 + (8 * g + q) * 128 + ((j ^ ((q >> 1) | ((g & 1) << 1))) * 32) + 8 * p;
      const bf16x8 ones = __builtin_bit_cast(bf16x8, u32x4{0x3f803f80u, 0x3f803f80u, 0x3f803f80u, 0x3f803f80u});
      auto csum = [&](unsigned so, f32x4 (&c)[2]) {
        #pragma unroll
        for (int k = 0; k < 4; k++) { unsigned a = ca + so + (k & 1) * 8192 + (k >> 1) * 4096; c[k & 1] = MFMA16(ones, tr2(a, a + 512), c[k & 1]); }
        wait_lgkm0m();
      };
      const unsigned FLA = l0 + L_S;
      issueW(0, 0); issueX(0, 0);
      const char* tp; unsigned tstep;
      {
        const int s0 = ((xcd & 3) * 27) >> 2, ns = (((xcd & 3) * 27 + 26) >> 2) - s0 + 1, n = (r * 4 + j) * 19 + lane;
        const int m = n - 1024, k = min(m / (2 * ns), 63), c = m % (2 * ns);
        tp = n < 1024 ? (const char*)x + (size_t)((xcd >> 2) * 1024 + n) * 1280 : (const char*)(c >= ns ? wu : wg) + (size_t)k * 3456 + (s0 + (c >= ns ? c - ns : c)) * 128;
        tstep = n < 1024 ? 128 : 64 * 3456;
      }
      auto g1_touch = [&](int s) { if (lane < (s < 10 ? 19 : 1)) touch(l0 + L_DUMP, tp + (size_t)(s < 10 ? s : 0) * tstep); };
      issueW(1, 2 * SLOT); issueX(1, 2 * SLOT);
      g1_touch(2); g1_touch(3);
      GET_TAG(14); bar();
      issueW(2, SLOT); issueX(2, SLOT);
      f32x4 cl[2] = {};
      bool faok = false;
      auto wd_touch = [&](int i) {
        const int nsh = G2MAP ? 8 : 4, nl = G2MAP ? 23 : 45, n = min((((rt & (nsh - 1)) * 3 + i) * 4 + j) * nl + min(lane, nl - 1), 2159);
        touch1(l0 + L_DUMP, faok ? O_WDT + (n / 40) * 40960 + cs * 5120 + (n % 40) * 128 : O_FA, ws);
      };
      auto get_stats = [&]() { touch1(l0 + L_ST + j * 256, O_STAT + (panel * 256 + j * 64 + lane) * 4, ws); };
      #pragma unroll
      for (int s = 0; s < 8; s++) {
        if (s) {
          wait_vm<16>(); bar();
          const unsigned so = ((s + 2) % 3 == 0 ? 0 : (s + 2) % 3 == 1 ? 2 : 1) * SLOT;
          issueW(s + 2, so); issueX(s + 2, so);
        }
        if (s < 7) {
          const int t0 = TL ? 4 + 2 * s : 4 + s, t1 = TL ? 5 + 2 * s : 10;
          if (s == 4 && j == 3) { if (lane < 20) glds1(FLA, O_FA + lane * 16, ws); } else g1_touch(t1);
          g1_touch(t0);
        } else {
          const u32x4 v = *(volatile LDS u32x4*)(size_t)(FLA + (lane < 20 ? lane : 0) * 16);
          faok = __all(v[0] == (unsigned)tag && v[2] == (unsigned)tag && v[1] == (unsigned)(tag >> 32) && v[3] == (unsigned)(tag >> 32));
          get_stats(); wd_touch(0);
        }
        csum((s % 3 == 0 ? 0 : s % 3 == 1 ? 2 : 1) * SLOT, cl);
      }
      wait_vm<16>(); bar();
      wd_touch(1); wd_touch(2);
      csum(SLOT, cl);
      wait_vm<4>();
      ST(1);
      bar();
      csum(0, cl);
      *(LDS float*)(size_t)(l0 + L_CS + j * 128 + (lane & 31) * 4) = (g & 1) ? cl[1][0] : cl[0][0];
      if (!faok) { poll(ws, O_FA + (lane < 40 ? lane : 0) * 8, tag); get_stats(); }
      wait_vm<0>();
      wait_lgkm0m();
      lds_put(l0 + L_RDY + 4 * j, 1);
      const int pos = 3 * gi + j;
      const bool hasc = j < 3 && pos < 640;
      const unsigned dvb = *(const GAS u16*)(dsk + pos2ch(hasc ? pos : 0));
      ST(2);
      if (j == 3) {
        wd_to_lds(10, 20);
        lds_wait4<4>(l0 + L_RDY, 1);
        lds_wait4<4>(l0 + L_FREE, 1);
        wd_to_lds(0, 10);
      } else if (hasc) {
        const unsigned KB = l0 + L_CONV + j * CONVB, UB = KB + KXB;
        const char* ub = ws + O_XNB + pos * 16;
        #pragma unroll
        for (int i = 0; i < 4; i++) glds1(UB + 512 + i * 1024, lane * 10240, ub + (size_t)i * 64 * 10240);
        *(LDS u32x2*)(size_t)(UB + lane * 8) = u32x2{0, 0};
        poll(ws, O_FK + pos * 8, tag);
        const u32x4 k01 = __builtin_amdgcn_raw_buffer_load_b128(rw, O_KX + pos * KCB + lane * 16, 0, SC1);
        const u32x2 k2 = __builtin_amdgcn_raw_buffer_load_b64(rw, O_KX + pos * KCB + 1024 + lane * 8, 0, SC1);
        kexpand(k01, k2, KB, lane);
        wait_vm<0>(); wait_lgkm0m();
        lds_wait4<4>(l0 + L_FREE, 1);
        ST(4);
        __builtin_amdgcn_s_setprio(1);
        conv(KB, UB, bf_lo(dvb), rw, O_ST + pos2ch(pos) * 4096, lane);
        ST(5);
        publish(ws, O_FS + pos2ch(pos) * 8, tag, lane);
        ST(3);
      }
    } else {
      if (threadIdx.x < 16) *(volatile LDS unsigned*)(size_t)(l0 + L_FULL + 4 * lane) = 0;
      unsigned wa[4], xa[2], wb[4], xb[2];
      #pragma unroll
      for (int u = 0; u < 4; u++) wa[u] = l0 + 32768 + (8 * g + q) * 128 + ((u ^ ((q >> 1) | ((g & 1) << 1))) * 32) + 8 * p;
      #pragma unroll
      for (int k = 0; k < 2; k++) xa[k] = l0 + (w * 64 + (lane & 15)) * 128 + (((4 * k + g) ^ ((lane >> 1) & 7)) * 16);
      f32x4 acc[8][4];
      #pragma unroll
      for (int f = 0; f < 8; f++)
        #pragma unroll
        for (int t = 0; t < 4; t++) { acc[f][t] = f32x4{0, 0, 0, 0}; asm volatile("" : "+v"(acc[f][t])); }
      bf16x8 X[2][4]; s16x4 W[4][2];
      auto rdx = [&](int k, int t) { X[k][t] = *(LDS bf16x8*)((LDS char*)(size_t)xb[k] + t * 2048); };
      auto rdw = [&](int n, int e) { const int k = n >> 3, f = n & 7; W[n & 3][e] = __builtin_amdgcn_ds_read_tr16_b64_v4i16((LDS s16x4*)((LDS char*)(size_t)wb[f >> 1] + (f & 1) * 8192 + k * 4096 + e * 512)); };
      auto mf = [&](int n, int t) { const int k = n >> 3, f = n & 7, tt = (f & 1) ? 3 - t : t;
        mf16(acc[f][tt], __builtin_bit_cast(bf16x8, __builtin_shufflevector(W[n & 3][0], W[n & 3][1], 0, 1, 2, 3, 4, 5, 6, 7)), X[k][tt]); };
      auto base = [&](unsigned& d, unsigned a, unsigned so) { asm volatile("v_add_u32 %0, %1, %2" : "=v"(d) : "i"(so), "v"(a)); };
      auto body = [&](unsigned son) {
        #pragma unroll
        for (int n = 0; n < 13; n++) {
          mf(n, 0); rdw(n + 3, 0); mf(n, 1); rdw(n + 3, 1); mf(n, 2);
          if (n < 4) rdx(1, n);
          if (n == 4) base(xb[0], xa[0], son);
          if (n == 5) base(xb[1], xa[1], son);
          if (n == 7) base(wb[0], wa[0], son);
          if (n == 9) base(wb[1], wa[1], son);
          if (n == 11) base(wb[2], wa[2], son);
          mf(n, 3);
        }
        base(wb[3], wa[3], son);
      };
      #pragma unroll
      for (int u = 0; u < 4; u++) wb[u] = wa[u];
      xb[0] = xa[0]; xb[1] = xa[1];
      GET_TAG(0);
      wait_lgkm0m();
      bar();
      ST(1);
      #pragma unroll
      for (int t = 0; t < 4; t++) rdx(0, t);
      #pragma unroll
      for (int n = 0; n < 3; n++) { rdw(n, 0); rdw(n, 1); }
      body(2 * SLOT);
      #pragma unroll
      for (int s = 1; s < 10; s++) {
        sb(); wait_lgkm0(); bar();
        mf(13, 0); rdw(0, 0); mf(13, 1); rdw(0, 1); mf(13, 2); rdx(0, 0); mf(13, 3); rdx(0, 1);
        mf(14, 0); rdw(1, 0); mf(14, 1); rdw(1, 1); mf(14, 2); rdx(0, 2); mf(14, 3); rdx(0, 3);
        mf(15, 0); rdw(2, 0); mf(15, 1); rdw(2, 1); mf(15, 2); mf(15, 3);
        body(((s + 1) % 3 == 0 ? 0 : (s + 1) % 3 == 1 ? 2 : 1) * SLOT);
      }
      sb(); wait_lgkm0();
      lds_put(l0 + L_FREE + 4 * w, 1);
      #pragma unroll
      for (int n = 13; n < 16; n++) { mf(n, 0); mf(n, 1); mf(n, 2); mf(n, 3); }
      __builtin_amdgcn_s_setprio(2);
      ST(2);
      lds_wait4<1>(l0 + L_RDY, 1);
      float nm[4], k0[4], k1[4], sq[4];
      #pragma unroll
      for (int t = 0; t < 4; t++) {
        unsigned st = *(LDS unsigned*)(size_t)(l0 + L_ST + (w * 64 + t * 16 + (lane & 15)) * 4);
        float den = bf_hi(st), ri = __builtin_amdgcn_rcpf(den);
        nm[t] = -bf_lo(st); k0[t] = -2.3022082f * ri; k1[t] = -2.3022082f * 0.044715f * ri * ri * ri; sq[t] = den * den;
      }
      #pragma unroll
      for (int f = 0; f < 8; f++) asm volatile("s_nop 0" : "+v"(acc[f][0]), "+v"(acc[f][1]), "+v"(acc[f][2]), "+v"(acc[f][3]));
      #pragma unroll
      for (int u = 0; u < 4; u++) {
        const f32x4 cg = *(LDS f32x4*)(size_t)(l0 + L_CS + u * 128 + g * 16), cu = *(LDS f32x4*)(size_t)(l0 + L_CS + u * 128 + 64 + g * 16);
        unsigned o[4][2];
        #pragma unroll
        for (int t = 0; t < 4; t++)
          #pragma unroll
          for (int e = 0; e < 2; e++) {
            f32x2 gg = f32x2{cg[2 * e], cg[2 * e + 1]} * nm[t] + f32x2{acc[2 * u][t][2 * e], acc[2 * u][t][2 * e + 1]};
            f32x2 uu = f32x2{cu[2 * e], cu[2 * e + 1]} * nm[t] + f32x2{acc[2 * u + 1][t][2 * e], acc[2 * u + 1][t][2 * e + 1]};
            f32x2 a = gg * (gg * gg * k1[t] + k0[t]);
            f32x2 ee = {__builtin_amdgcn_exp2f(a[0]), __builtin_amdgcn_exp2f(a[1])};
            ee = ee * sq[t] + sq[t];
            f32x2 hv = gg * uu * f32x2{__builtin_amdgcn_rcpf(ee[0]), __builtin_amdgcn_rcpf(ee[1])};
            o[t][e] = pk2(hv[0], hv[1]);
          }
        #pragma unroll
        for (int k = 0; k < 2; k++) {
          auto r0 = __builtin_amdgcn_permlane16_swap(o[2 * k][0], o[2 * k + 1][0], false, false), r1 = __builtin_amdgcn_permlane16_swap(o[2 * k][1], o[2 * k + 1][1], false, false);
          __builtin_amdgcn_raw_buffer_store_b128(u32x4{r0[0], r1[0], r0[1], r1[1]}, rw, O_HT + (((panel * 8 + w * 2 + k) * 108 + slice * 4 + u) * 64 + lane) * 16, 0, SC1);
        }
      }
      __builtin_amdgcn_s_setprio(0);
      ST(3);
      publish(ws, O_FH + ((panel * 4 + w) * 32 + slice) * 8, tag, lane);
      ST(4);
    }
  } else {
    const int ai = xcd * 5 + r - 27;
    const unsigned S = l0 + w * AUXW;
    const int tb = w * 40 + ai, aw = ai * 8 + w;
    const bool hasln = tb < 256;
    const int job = (7 - w) * 40 + ai;
    if (job < 270) {
      const char* rb = (const char*)wd + (size_t)(job / 5) * 32 * 1280 + (job % 5) * 256;
      #pragma unroll
      for (int i = 0; i < 8; i++) glds(S + i * 1024, (lane >> 4) * 1280 + (((lane & 15) ^ (2 * ((lane >> 4) | (i & 2) << 1))) * 16), rb + i * 4 * 1280);
    }
    int pfk[2]; const char* pfp[2];
    #pragma unroll
    for (int i = 0; i < 2; i++) {
      int n = 54 * 64 + ((i * 8 + 7 - w) * 40 + ai) * 64 + lane, k = n / 54, c = n - k * 54;
      pfk[i] = k; pfp[i] = (const char*)(c >= 27 ? wu : wg) + (size_t)k * 3456 + (c >= 27 ? c - 27 : c) * 128;
    }
    unsigned xv[8][5];
    if (hasln) {
      #pragma unroll
      for (int i = 0; i < 8; i++)
        #pragma unroll
        for (int jj = 0; jj < 5; jj++) xv[i][jj] = ((const unsigned*)x)[(tb * 8 + i) * 320 + jj * 64 + lane];
    }
    #pragma unroll
    for (int i = 0; i < 2; i++)
      if (pfk[i] < 640) touch(l0 + L_DUMP, pfp[i]);
    KPar P0 = kpar_load(lar, aim, cre, cim, ldt, pos2ch(aw), lane), P1 = kpar_load(lar, aim, cre, cim, ldt, pos2ch(aw + 320), lane);
    ST(9);
    if (hasln) {
      float s8[8];
      const bf16x2 one2 = __builtin_bit_cast(bf16x2, 0x3f803f80u);
      #pragma unroll
      for (int i = 0; i < 8; i++) { float s = 0;
        #pragma unroll
        for (int jj = 0; jj < 5; jj++) s = __builtin_amdgcn_fdot2_f32_bf16(__builtin_bit_cast(bf16x2, xv[i][jj]), one2, s, false);
        s8[i] = s; }
      const float mean = red8(s8, lane) * (1.f / D);
      f32x2 dv[8][5];
      #pragma unroll
      for (int i = 0; i < 8; i++) {
        const float mi = rdl(mean, red8_lane(i));
        f32x2 qq = {0, 0};
        #pragma unroll
        for (int jj = 0; jj < 5; jj++) { dv[i][jj] = f32x2{bf_lo(xv[i][jj]), bf_hi(xv[i][jj])} - mi; qq += dv[i][jj] * dv[i][jj]; }
        s8[i] = qq[0] + qq[1];
      }
      const float var = rbf(red8(s8, lane) * (1.f / D));
      const float den = rbf(__builtin_sqrtf(rbf(var + 1e-5f))), mb = rbf(mean);
      const float ri = 1.f / den, off = (mean - mb) * ri;
      if ((lane & 7) == 0) __builtin_amdgcn_raw_buffer_store_b32(pk2(mb, den), rw, O_STAT + (tb * 8 + ((lane >> 5) | (((lane >> 4) & 1) << 1) | (((lane >> 3) & 1) << 2))) * 4, 0, SC1);
      #pragma unroll
      for (int i = 0; i < 8; i++) {
        const float rr = rdl(ri, red8_lane(i)), oo = rdl(off, red8_lane(i));
        #pragma unroll
        for (int jj = 0; jj < 5; jj++) dv[i][jj] = dv[i][jj] * rr + oo;
      }
      #pragma unroll
      for (int jj = 0; jj < 5; jj++)
        #pragma unroll
        for (int e = 0; e < 2; e++)
          __builtin_amdgcn_raw_buffer_store_b128(u32x4{pk2(dv[0][jj][e], dv[1][jj][e]), pk2(dv[2][jj][e], dv[3][jj][e]), pk2(dv[4][jj][e], dv[5][jj][e]), pk2(dv[6][jj][e], dv[7][jj][e])},
                                                 rw, O_XNB + (tb * 640 + jj * 128 + e * 64 + lane) * 16, 0, SC1);
    }
    ST(10);
    if (job < 270) {
      if (!hasln) wait_vm<0>();
      #pragma unroll
      for (int c = 0; c < 8; c++) {
        unsigned a = S + (8 * g + q) * 256 + ((c ^ (q | (g & 1) << 2)) * 32) + 8 * p;
        __builtin_amdgcn_raw_buffer_store_b128(__builtin_bit_cast(u32x4, tr2(a, a + 1024)), rw, O_WDT + (((job / 5) * 40 + (job % 5) * 8 + c) * 64 + lane) * 16, 0, SC1);
      }
    }
    GET_TAG(0);
    bar();
    if (w == 0) publish(ws, O_FA + ai * 8, tag, lane);
    ST(2);
    #pragma unroll 1
    for (int c = 0; c < 2; c++) {
      kgen(c ? P1 : P0, S, rw, O_KX + (aw + 320 * c) * KCB, lane);
      ST(13 + c);
      publish(ws, O_FK + (aw + 320 * c) * 8, tag, lane);
    }
    ST(3);
    if (w == 7) lds_put(l0 + L_FLAG, 0);
    wait_lgkm0m();
    bar();
    if (w == 7 || w < 4) poll(ws, O_FA + (lane < 40 ? lane : 0) * 8, tag);
    if (w == 7) wd_to_lds(0, 20);
  }

  if (w == 7) {
    poll(ws, O_FH + (rt * 32 + (lane < 27 ? lane : 0)) * 8, tag);
    lds_put(l0 + L_FLAG, 1);
    ST(5);
    poll(ws, O_FS + (cs * 80 + lane) * 8, O_FS + (cs * 80 + 64 + (lane & 15)) * 8, tag);
    #pragma unroll
    for (int i = 0; i < 10; i++) glds1(l0 + L_S + i * 1024, (lane >> 3) * 4096 + (((lane & 7) ^ (2 * ((lane >> 4) & 3))) * 16), ws + O_ST + (size_t)(cs * 80 + i * 8) * 4096 + rt * 128);
    if (b == 255) {
      #pragma unroll 1
      for (int i = 0; i < 4; i++) { int t = i * 64 + lane; poll(ws, t < 216 ? O_FH + ((t / 27) * 128 + t % 27) * 8 : O_FA + (t - 216) * 8, tag); }
      if (lane == 0) __hip_atomic_store((GAS u64*)(ws + O_E), tag, RLX);
    }
    wait_vm<0>();
    ST(6);
    bar();
  } else if (w >= 4) {
    bar();
  } else {
    const unsigned hb = O_HT + ((rt * 2 * 108 + (lane >> 5)) * 64 + ((lane >> 4) & 1) * 32 + (lane & 15)) * 16;
    const unsigned wb = O_WDT + (cs * 5 * 64 + lane) * 16;
    unsigned ks[14];
    #pragma unroll
    for (int i = 0; i < 14; i++) ks[i] = (i == 13 && w >= 2) ? 0x4000u : kidx(w, i);
    auto ldH = [&](int i, int tt) { return __builtin_bit_cast(bf16x8, __builtin_amdgcn_raw_buffer_load_b128(rw, hb + (tt & 1) * 256, ks[i] * 2048 + (tt >> 1) * 110592, SC1)); };
    auto ldW = [&](int i, int c) { return __builtin_bit_cast(bf16x8, __builtin_amdgcn_raw_buffer_load_b128(rw, wb + c * 1024, ks[i] * 40960, SC1)); };
    bf16x8 W[4][5], H[4][4];
    #pragma unroll
    for (int i = 0; i < 4; i++)
      #pragma unroll
      for (int c = 0; c < 5; c++) W[i][c] = ldW(i, c);
    sb();
    lds_wait(l0 + L_FLAG);
    ST(5);
    constexpr int ord[14] = {0, 4, 1, 5, 2, 6, 3, 7, 9, 8, 10, 11, 12, 13};
    constexpr int set[14] = {0, 0, 1, 1, 2, 2, 3, 3, 0, 0, 1, 2, 3, 0};
    #pragma unroll
    for (int i = 0; i < 3; i++)
      #pragma unroll
      for (int t = 0; t < 4; t++) H[i][t] = ldH(ord[i], t);
    f32x4 acc[4][5];
    u32x2 xr[5];
    const unsigned wl = l0 + L_WD + w * WDW + lane * 16;
    auto kstep = [&](int i) {
      const bool inl = ord[i] >= 4 && ord[i] < 9, nxl = i < 13 && ord[i + 1] >= 4 && ord[i + 1] < 9;
      #pragma unroll
      for (int c = 0; c < 5; c++) {
        #pragma unroll
        for (int t = 0; t < 4; t++) {
          const int tt = (c & 1) ? 3 - t : t;
          if (i == 0) mf16z(acc[tt][c], W[set[i]][c], H[i & 3][tt]); else mf16(acc[tt][c], W[set[i]][c], H[i & 3][tt]);
          if (t == 0 && c < 4 && i + 3 < 14) H[(i + 3) & 3][c] = ldH(ord[i + 3], c);
        }
        if (nxl) W[set[i]][c] = ld128(wl + (ord[i + 1] - 4) * 5120 + c * 1024);
        else if (inl) W[set[i]][c] = ldW(ord[i] + 5, c);
        else if (i == 10) xr[c] = *(const u32x2*)(x + (size_t)(rt * 64 + w * 16 + (lane & 15)) * D + cs * 80 + c * 16 + (lane >> 4) * 4);
      }
    };
    #pragma unroll
    for (int i = 0; i < 13; i++) kstep(i);
    if (w < 2) kstep(13);
    mf_pad();
    sb();
    ST(6);
    #pragma unroll
    for (int t = 0; t < 4; t++)
      #pragma unroll
      for (int c = 0; c < 5; c++) *(LDS f32x4*)(size_t)(wl + (t * 5 + c) * 1024) = acc[t][c];
    wait_lgkm0m();
    bar();
    ST(7);
    #pragma unroll
    for (int c = 0; c < 5; c++) {
      f32x4 o = {0, 0, 0, 0};
      #pragma unroll
      for (int v = 0; v < 4; v++) o += *(LDS f32x4*)(size_t)(l0 + L_WD + v * WDW + (w * 5 + c) * 1024 + lane * 16);
      const int row = c * 16 + 4 * g + q;
      u32x2 sv = __builtin_bit_cast(u32x2, trd(l0 + L_S + row * 128 + ((w ^ ((row >> 1) & 3)) * 32) + 8 * p));
      float sf[4] = {bf_lo(sv[0]), bf_hi(sv[0]), bf_lo(sv[1]), bf_hi(sv[1])}, xf[4] = {bf_lo(xr[c][0]), bf_hi(xr[c][0]), bf_lo(xr[c][1]), bf_hi(xr[c][1])}, y[4];
      #pragma unroll
      for (int e = 0; e < 4; e++) y[e] = fmaf(sf[e], o[e] * __builtin_amdgcn_rcpf(1.f + __builtin_amdgcn_exp2f(-1.4426950408889634f * o[e])), xf[e]);
      *(u32x2*)(out + (size_t)(rt * 64 + w * 16 + (lane & 15)) * D + cs * 80 + c * 16 + g * 4) = u32x2{pk2(y[0], y[1]), pk2(y[2], y[3])};
    }
    ST(8);
  }
  ST_DUMP();
}

extern "C" void kernel_launch(void* const* d_in, const int* in_sizes, int n_in, void* d_out, int out_size, void* d_ws, size_t ws_size, hipStream_t stream) {
  (void)hipFuncSetAttribute((const void*)fused_kernel, hipFuncAttributeMaxDynamicSharedMemorySize, LDS_BYTES);
  fused_kernel<<<256, 512, LDS_BYTES, stream>>>((const u16*)d_in[0], (const u16*)d_in[1], (const u16*)d_in[2], (const u16*)d_in[3], (const u16*)d_in[4], (const u16*)d_in[5],
      (const u16*)d_in[6], (const u16*)d_in[8], (const u16*)d_in[9], (const u16*)d_in[10], (u16*)d_out, (char*)d_ws);
}
```

```cpp
#include <hip/hip_runtime.h>
#include <stdint.h>

typedef unsigned short u16;
typedef unsigned long long u64;
typedef __attribute__((ext_vector_type(4))) short s16x4;
typedef __attribute__((ext_vector_type(8))) __bf16 bf16x8;
typedef __attribute__((ext_vector_type(2))) __bf16 bf16x2;
typedef __attribute__((ext_vector_type(2))) float f32x2;
typedef __attribute__((ext_vector_type(4))) float f32x4;
typedef __attribute__((ext_vector_type(16))) float f32x16;
typedef __attribute__((ext_vector_type(2))) unsigned u32x2;
typedef __attribute__((ext_vector_type(4))) unsigned u32x4;
#define LDS __attribute__((address_space(3)))
#define GAS __attribute__((address_space(1)))
#define DI __device__ __forceinline__
#define RLX __ATOMIC_RELAXED, __HIP_MEMORY_SCOPE_AGENT
#define SC1 16

constexpr int L = 2048, D = 640, F = 1728;
constexpr unsigned O_E = 0, O_FA = 1024, O_FK = 2048, O_FH = 8192  , O_FS = 16384;
constexpr unsigned O_STAT = 524288, O_XNB = 1u << 20, O_WDT = 4u << 20, O_KX = 7u << 20, O_HT = 13u << 20, O_ST = 21u << 20, WS_BYTES = 24u << 20;
constexpr unsigned KXB = 8448, KCB = 1536;
constexpr unsigned SLOT = 49152;
constexpr unsigned L_WD = 0, WDW = 25600;
constexpr unsigned L_CONV = 102400, CONVB = 13056;
constexpr unsigned L_S = 147456;
constexpr unsigned L_CS = 159744;
constexpr unsigned L_ST = 160256;
constexpr unsigned L_FULL = 161280, L_FREE = L_FULL + 16, L_RDY = L_FULL + 32, L_FLAG = L_FULL + 48, L_DUMP = L_FULL + 256;
constexpr unsigned LDS_BYTES = 163840;
constexpr unsigned AUXW = 20480;
#define SPL 20000
constexpr int SPIN_LIMIT = SPL;

#define ST(i) do {} while (0)
#define ST_INIT() do {} while (0)
#define ST_DUMP() do {} while (0)

DI float bf_lo(unsigned v) { return __uint_as_float(v << 16); }
DI float bf_hi(unsigned v) { return __uint_as_float(v & 0xffff0000u); }
DI unsigned pk2(float a, float b) { return __builtin_bit_cast(unsigned, __builtin_convertvector(f32x2{a, b}, bf16x2)); }
DI float rbf(float a) { return bf_lo(pk2(a, a) & 0xffffu); }

DI void glds(unsigned lds, unsigned voff, const char* sbase) {
  asm volatile("s_mov_b32 m0, %0\n\ts_nop 0\n\tglobal_load_lds_dwordx4 %1, %2" :: "s"(lds), "v"(voff), "s"(sbase) : "memory");
}
DI void glds1(unsigned lds, unsigned voff, const char* sbase) {
  asm volatile("s_mov_b32 m0, %0\n\ts_nop 0\n\tglobal_load_lds_dwordx4 %1, %2 sc1" :: "s"(lds), "v"(voff), "s"(sbase) : "memory");
}
DI void touch(unsigned lds, const char* p) { asm volatile("s_mov_b32 m0, %0\n\ts_nop 0\n\tglobal_load_lds_dword %1, off" :: "s"(lds), "v"(p) : "memory"); }
DI void touch1(unsigned lds, unsigned voff, const char* sbase) { asm volatile("s_mov_b32 m0, %0\n\ts_nop 0\n\tglobal_load_lds_dword %1, %2 sc1" :: "s"(lds), "v"(voff), "s"(sbase) : "memory"); }
template<int N> DI void wait_vm() { asm volatile("s_waitcnt vmcnt(%0)" :: "i"(N) : "memory"); }
template<int B> DI void wait_vmx(int n) {
  if (n == 0) wait_vm<B>();
  else if (n < 3) { if (n == 1) wait_vm<B + 1>(); else wait_vm<B + 2>(); }
  else if (n < 5) { if (n == 3) wait_vm<B + 3>(); else wait_vm<B + 4>(); }
  else if (n == 5) wait_vm<B + 5>(); else wait_vm<B + 6>();
}
DI void sb() { __builtin_amdgcn_sched_barrier(0); }
DI void bar() { sb(); __builtin_amdgcn_s_barrier(); sb(); }
DI void wait_lgkm0() { __builtin_amdgcn_s_waitcnt(0xC07F); }
DI void wait_lgkm0m() { asm volatile("s_waitcnt lgkmcnt(0)" ::: "memory"); }
DI s16x4 trd(unsigned a) { return __builtin_amdgcn_ds_read_tr16_b64_v4i16((LDS s16x4*)(size_t)a); }
DI bf16x8 tr2(unsigned a, unsigned b) { return __builtin_bit_cast(bf16x8, __builtin_shufflevector(trd(a), trd(b), 0, 1, 2, 3, 4, 5, 6, 7)); }
DI bf16x8 ld128(unsigned a) { return *(LDS bf16x8*)(size_t)a; }
#define MFMA32(a, b, c) __builtin_amdgcn_mfma_f32_32x32x16_bf16(a, b, c, 0, 0, 0)
#define MFMA16(a, b, c) __builtin_amdgcn_mfma_f32_16x16x32_bf16(a, b, c, 0, 0, 0)
#define SGB(m, n) __builtin_amdgcn_sched_group_barrier(m, n, 0)
typedef __amdgpu_buffer_rsrc_t rsrc_t;
DI void mf16(f32x4& c, const bf16x8& a, const bf16x8& b) { asm volatile("v_mfma_f32_16x16x32_bf16 %0, %1, %2, %0" : "+v"(c) : "v"(a), "v"(b)); }
DI void mf16z(f32x4& c, const bf16x8& a, const bf16x8& b) { asm volatile("v_mfma_f32_16x16x32_bf16 %0, %1, %2, 0" : "=&v"(c) : "v"(a), "v"(b)); }
DI void mf_pad() { asm volatile("s_nop 7\n\ts_nop 1" ::: "memory"); }
typedef u32x2 __attribute__((aligned(4))) u32x2a;

DI void poll(const char* ws, unsigned off, unsigned off2, u64 tag) {
  #pragma unroll 1
  for (int it = 0; it < SPIN_LIMIT; it++) {
    const u64 v = __hip_atomic_load((const GAS u64*)(ws + off), RLX), v2 = __hip_atomic_load((const GAS u64*)(ws + off2), RLX);
    if (__all(v == tag && v2 == tag)) break;
    __builtin_amdgcn_s_sleep(2);
  }
  asm volatile("" ::: "memory");
}
DI void poll(const char* ws, unsigned off, u64 tag) {
  #pragma unroll 1
  for (int it = 0; it < SPIN_LIMIT; it++) {
    if (__all(__hip_atomic_load((const GAS u64*)(ws + off), RLX) == tag)) break;
    __builtin_amdgcn_s_sleep(2);
  }
  asm volatile("" ::: "memory");
}
DI void publish(char* ws, unsigned off, u64 tag, int lane) {
  asm volatile("s_waitcnt vmcnt(0)" ::: "memory");
  if (lane == 0) __hip_atomic_store((GAS u64*)(ws + off), tag, RLX);
}
DI void lds_wait(unsigned a) {
  #pragma unroll 1
  for (int it = 0; it < 16 * SPIN_LIMIT; it++) {
    if (__builtin_amdgcn_readfirstlane(*(volatile LDS unsigned*)(size_t)a)) break;
    __builtin_amdgcn_s_sleep(1);
  }
  asm volatile("" ::: "memory");
}
DI unsigned min4(u32x4 v) { return __builtin_amdgcn_readfirstlane(min(min(v[0], v[1]), min(v[2], v[3]))); }
DI unsigned lds_min4(unsigned a) { return min4(*(volatile LDS u32x4*)(size_t)a); }
template<int SL> DI void lds_wait4(unsigned a, unsigned need) {
  #pragma unroll 1
  for (int it = 0; it < 16 * SPIN_LIMIT && lds_min4(a) < need; it++) __builtin_amdgcn_s_sleep(SL);
  asm volatile("" ::: "memory");
}
DI void lds_put(unsigned a, unsigned v) { if ((threadIdx.x & 63) == 0) *(volatile LDS unsigned*)(size_t)a = v; }
DI int pos2ch(int pos) { return (pos & ~127) + 2 * (pos & 63) + ((pos >> 6) & 1); }

DI float red8(const float (&a)[8], int lane) {
  float b[4], c[2];
  #pragma unroll
  for (int i = 0; i < 4; i++) { auto r = __builtin_amdgcn_permlane32_swap(__float_as_uint(a[2 * i]), __float_as_uint(a[2 * i + 1]), false, false); b[i] = __uint_as_float(r[0]) + __uint_as_float(r[1]); }
  #pragma unroll
  for (int i = 0; i < 2; i++) { auto r = __builtin_amdgcn_permlane16_swap(__float_as_uint(b[2 * i]), __float_as_uint(b[2 * i + 1]), false, false); c[i] = __uint_as_float(r[0]) + __uint_as_float(r[1]); }
  bool up = lane & 8;
  float keep = up ? c[1] : c[0], send = up ? c[0] : c[1];
  float d = keep + __uint_as_float(__builtin_amdgcn_update_dpp(0, __float_as_uint(send), 0x128, 0xf, 0xf, true));
  d += __uint_as_float(__builtin_amdgcn_update_dpp(0, __float_as_uint(d), 0x141, 0xf, 0xf, true));
  d += __uint_as_float(__builtin_amdgcn_update_dpp(0, __float_as_uint(d), 0xB1, 0xf, 0xf, true));
  d += __uint_as_float(__builtin_amdgcn_update_dpp(0, __float_as_uint(d), 0x4E, 0xf, 0xf, true));
  return d;
}
DI constexpr int red8_lane(int i) { return ((i & 1) << 5) | (((i >> 1) & 1) << 4) | (((i >> 2) & 1) << 3); }
DI float rdl(float v, int l) { return __uint_as_float(__builtin_amdgcn_readlane(__float_as_uint(v), l)); }

DI f32x2 cmul(f32x2 a, f32x2 b) {
  f32x2 r;
  asm("v_pk_mul_f32 %0, %1, %2 op_sel:[1,1] op_sel_hi:[1,0] neg_lo:[0,1]\n\tv_pk_fma_f32 %0, %1, %2, %0 op_sel_hi:[0,1,1]" : "=&v"(r) : "v"(a), "v"(b));
  return r;
}
DI f32x2 zpow(float al, float be, float p) {
  float e = __builtin_amdgcn_exp2f(al * p), f = __builtin_amdgcn_fractf(be * p);
  return f32x2{e * __builtin_amdgcn_cosf(f), e * __builtin_amdgcn_sinf(f)};
}

typedef __attribute__((ext_vector_type(2))) _Float16 h2;
typedef __attribute__((ext_vector_type(8))) _Float16 f16x8;
DI h2 cmulh(h2 a, h2 b) { h2 t = h2{a[1], a[1]} * h2{-b[1], b[0]}; return __builtin_elementwise_fma(h2{a[0], a[0]}, b, t); }
DI h2 toh(f32x2 a) { return __builtin_convertvector(a, h2); }
DI h2 tohc(f32x2 a) { h2 r; asm("v_cvt_pk_f16_f32 %0, %1, -%2" : "=v"(r) : "v"(a[0]), "v"(a[1])); return r; }
template<int C> DI float maxd(float v) { return fmaxf(v, __uint_as_float(__builtin_amdgcn_update_dpp(0, __float_as_uint(v), C, 0xf, 0xf, true))); }

struct KPar { unsigned lar, aim, cre, cim; unsigned ldt; };
DI KPar kpar_load(const u16* lar, const u16* aim, const u16* cre, const u16* cim, const u16* ldt, int ch, int lane) {
  KPar p; int o = ch * 64 + lane;
  p.lar = ((const unsigned*)lar)[o]; p.aim = ((const unsigned*)aim)[o]; p.cre = ((const unsigned*)cre)[o]; p.cim = ((const unsigned*)cim)[o];
  p.ldt = ldt[ch];
  return p;
}
DI void kgen(const KPar& P, unsigned S, rsrc_t rw, unsigned goff, int lane) {
  const float LOG2E = 1.4426950408889634f;
  const float dt = rbf(__builtin_amdgcn_exp2f(bf_lo(P.ldt) * LOG2E));
  f32x2 C[2] = {f32x2{bf_lo(P.cre), bf_lo(P.cim)}, f32x2{bf_hi(P.cre), bf_hi(P.cim)}};
  float mx = fmaxf(fmaxf(fabsf(C[0][0]), fabsf(C[0][1])), fmaxf(fabsf(C[1][0]), fabsf(C[1][1])));
  mx = maxd<0xB1>(mx); mx = maxd<0x4E>(mx); mx = maxd<0x141>(mx); mx = maxd<0x140>(mx);
  mx = fmaxf(fmaxf(rdl(mx, 0), rdl(mx, 16)), fmaxf(rdl(mx, 32), rdl(mx, 48)));
  const int ex = __builtin_amdgcn_frexp_expf(mx);
  const float cs = __builtin_amdgcn_ldexpf(1.f, -ex), ks = __builtin_amdgcn_ldexpf(2.f * dt, ex);
  h2 T[2][8], Pw[2][4], Q[2][4], U[2][17];
  #pragma unroll
  for (int m = 0; m < 2; m++) {
    float la = m ? bf_hi(P.lar) : bf_lo(P.lar), ai = m ? bf_hi(P.aim) : bf_lo(P.aim);
    float al = -rbf(__builtin_amdgcn_exp2f(la * LOG2E)) * dt * LOG2E, be = ai * dt * 0.15915494309189535f;
    f32x2 z1 = zpow(al, be, 1.f), u1 = zpow(al, be, 128.f);
    f32x2 z2 = cmul(z1, z1), z3 = cmul(z2, z1), z4 = cmul(z2, z2), z8 = cmul(z4, z4), z16 = cmul(z8, z8), z24 = cmul(z16, z8), z32 = cmul(z16, z16);
    f32x2 q0 = C[m] * cs, q1 = cmul(q0, z32), q2 = cmul(q1, z32), q3 = cmul(q2, z32);
    f32x2 u2 = cmul(u1, u1), u3 = cmul(u2, u1), u4 = cmul(u2, u2), u8 = cmul(u4, u4), u12 = cmul(u8, u4), u16 = cmul(u8, u8);
    T[m][0] = h2{1, 0}; T[m][1] = toh(z1); T[m][2] = toh(z2); T[m][3] = toh(z3); T[m][4] = toh(z4);
    Pw[m][1] = tohc(z8); Pw[m][2] = tohc(z16); Pw[m][3] = tohc(z24);
    Q[m][0] = tohc(q0); Q[m][1] = tohc(q1); Q[m][2] = tohc(q2); Q[m][3] = tohc(q3);
    U[m][1] = toh(u1); U[m][2] = toh(u2); U[m][3] = toh(u3); U[m][4] = toh(u4); U[m][8] = toh(u8); U[m][12] = toh(u12); U[m][16] = toh(u16);
  }
  const unsigned wa = S + lane * 8, ra = S + (lane & 15) * 528 + (lane >> 4) * 16;
  auto put = [&](int row, h2 v0, h2 v1) { *(LDS u32x2*)((LDS char*)(size_t)wa + row * 528) = u32x2{__builtin_bit_cast(unsigned, v0), __builtin_bit_cast(unsigned, v1)}; };
  auto frag = [&](int k) { return *(LDS f16x8*)((LDS char*)(size_t)ra + k * 64); };
  f16x8 fine[8];
  #pragma unroll
  for (int b = 0; b < 16; b++) { if (b & 3) put(b, cmulh(Q[0][b >> 2], Pw[0][b & 3]), cmulh(Q[1][b >> 2], Pw[1][b & 3])); else put(b, Q[0][b >> 2], Q[1][b >> 2]); }
  #pragma unroll
  for (int k = 0; k < 8; k++) fine[k] = frag(k);
  f32x4 acc[3];
  auto tile = [&](int t) {
    f32x4 a = {0, 0, 0, 0}, a2 = {0, 0, 0, 0};
    #pragma unroll
    for (int k = 0; k < 8; k += 2) { a = __builtin_amdgcn_mfma_f32_16x16x32_f16(frag(k), fine[k], a, 0, 0, 0); a2 = __builtin_amdgcn_mfma_f32_16x16x32_f16(frag(k + 1), fine[k + 1], a2, 0, 0, 0); }
    acc[t] = (a + a2) * ks;
  };
  #pragma unroll
  for (int e = 5; e < 8; e++) { T[0][e] = cmulh(T[0][4], T[0][e - 4]); T[1][e] = cmulh(T[1][4], T[1][e - 4]); }
  #pragma unroll
  for (int e = 0; e < 8; e++) put(e, T[0][e], T[1][e]);
  put(8, U[0][1], U[1][1]);
  #pragma unroll
  for (int e = 1; e < 8; e++) put(8 + e, cmulh(T[0][e], U[0][1]), cmulh(T[1][e], U[1][1]));
  tile(0);
  #pragma unroll
  for (int a = 2; a < 4; a++) {
    put(4 * (a - 2), U[0][a], U[1][a]);
    #pragma unroll
    for (int e = 1; e < 4; e++) put(4 * (a - 2) + e, cmulh(T[0][2 * e], U[0][a]), cmulh(T[1][2 * e], U[1][a]));
  }
  #pragma unroll
  for (int a = 4; a < 8; a++) {
    if (a > 4) { U[0][a] = cmulh(U[0][4], U[0][a - 4]); U[1][a] = cmulh(U[1][4], U[1][a - 4]); }
    put(8 + 2 * (a - 4), U[0][a], U[1][a]);
    put(9 + 2 * (a - 4), cmulh(T[0][4], U[0][a]), cmulh(T[1][4], U[1][a]));
  }
  tile(1);
  #pragma unroll
  for (int a = 8; a <= 16; a++) { if (a & 3) put(a - 8, cmulh(U[0][a & ~3], U[0][a & 3]), cmulh(U[1][a & ~3], U[1][a & 3])); else put(a - 8, U[0][a], U[1][a]); }
  tile(2);
  __builtin_amdgcn_raw_buffer_store_b128(u32x4{pk2(acc[0][0], acc[0][1]), pk2(acc[0][2], acc[0][3]), pk2(acc[1][0], acc[1][1]), pk2(acc[1][2], acc[1][3])}, rw, goff + lane * 16, 0, SC1);
  __builtin_amdgcn_raw_buffer_store_b64(u32x2{pk2(acc[2][0], acc[2][1]), pk2(acc[2][2], acc[2][3])}, rw, goff + 1024 + lane * 8, 0, SC1);
}
DI unsigned prm(unsigned hi, unsigned lo, unsigned sel) { return __builtin_amdgcn_perm(hi, lo, sel); }
DI void kexpand(u32x4 k01, u32x2 k2, unsigned S, int lane) {
  const int b = lane & 15, g = lane >> 4;
  const unsigned TR = S + 4608;
  const unsigned LL = 0x01000100u, HH = 0x03020302u;
  unsigned a3 = g < 2 ? S + 8 + 2 * (2044 - (1024 + 512 * g + 384 + 8 * b)) : TR;
  unsigned a4 = (g < 2 && !(b & 1)) ? a3 - 2 : TR;
  unsigned v3[4];
  #pragma unroll
  for (int j = 0; j < 4; j++) { v3[j] = prm(0, k2[j >> 1], (j & 1) ? HH : LL); *(LDS u32x4*)((LDS char*)(size_t)a3 + 256 * (3 - j)) = u32x4{v3[j], v3[j], v3[j], v3[j]}; }
  *(LDS u32x4*)(size_t)(lane == 32 ? S : TR) = u32x4{v3[0], v3[0], v3[0], v3[0]};
  #pragma unroll
  for (int j = 0; j < 4; j++) *(volatile LDS u16*)((LDS char*)(size_t)a4 + 256 * (3 - j)) = (u16)v3[j];
  unsigned a2 = g >= 2 ? S + 8 + 2 * (2042 - (512 + 256 * (g - 2) + 128 + 8 * b)) : TR;
  const unsigned l0_ = prm(0, k01[2], LL), h0_ = prm(0, k01[2], HH), l1_ = prm(0, k01[3], LL), h1_ = prm(0, k01[3], HH);
  #pragma unroll
  for (int h = 0; h < 2; h++) {
    volatile LDS unsigned* q = (volatile LDS unsigned*)((LDS char*)(size_t)a2 + 256 * (1 - h));
    q[0] = h ? h1_ : h0_; q[1] = prm(0, k01[2 + h], 0x01000302u); q[2] = h ? l1_ : l0_; q[3] = h ? l1_ : l0_;
  }
  LDS char* a1 = (LDS char*)(size_t)(g < 2 ? S + 8 + 2 * (2041 - (256 + 128 * g + 8 * b)) : TR + 2);
  *(volatile LDS u16*)a1 = (u16)h1_;
  *(volatile LDS unsigned*)(a1 + 2) = l1_;
  *(volatile LDS unsigned*)(a1 + 6) = prm(k01[2], k01[3], 0x07060100u);
  *(volatile LDS unsigned*)(a1 + 10) = l0_;
  *(volatile LDS u16*)(a1 + 14) = (u16)l0_;
  unsigned a0 = S + 8 + 2 * (2044 - (128 * (g >> 1) + 4 * (g & 1) + 8 * b));
  *(volatile LDS u32x2*)(size_t)a0 = u32x2{prm(0, k01[1], 0x01000302u), prm(0, k01[0], 0x01000302u)};
  *(volatile LDS u32x2*)(size_t)(lane < 6 ? S + 8 + 4096 + 8 * lane : TR) = u32x2{0, 0};
  asm volatile("" ::: "memory");
  u32x2 x[5], y[5]; unsigned z[5];
  #pragma unroll
  for (int i = 0; i < 5; i++) {
    LDS char* a = (LDS char*)(size_t)(S + 8 + 16 * (i < 4 ? lane : (lane & 1))) + 1024 * i;
    x[i] = *(LDS u32x2*)a; y[i] = *(LDS u32x2*)(a + 8); z[i] = *(LDS unsigned*)(a + 16);
  }
  asm volatile("" ::: "memory");
  #pragma unroll
  for (int i = 0; i < 5; i++) {
    LDS char* a = (LDS char*)(size_t)(S + 4168 + 16 * (i < 4 ? lane : (lane & 1))) + 1024 * i;
    if (i < 4 || lane < 2) {
      *(LDS u32x2*)a = u32x2{__builtin_amdgcn_alignbit(x[i][1], x[i][0], 16), __builtin_amdgcn_alignbit(y[i][0], x[i][1], 16)};
      *(LDS u32x2*)(a + 8) = u32x2{__builtin_amdgcn_alignbit(y[i][1], y[i][0], 16), __builtin_amdgcn_alignbit(z[i], y[i][1], 16)};
    }
  }
  asm volatile("" ::: "memory");
}

DI void conv(unsigned KB, unsigned UB, float dsk, rsrc_t rw, unsigned goff, int lane) {
  const int r = lane & 15, kg = lane >> 4;
  const int n0 = 2047 - (16 * 112 + 16 * (kg >> 1) + r - 8 * (kg & 1));
  unsigned ka = KB + 8 + ((n0 & 1) ? 4160 + 2 * (n0 - 1) : 2 * n0);
  unsigned ua = UB + 2 * (256 + 16 * r - 16 * (kg >> 1) + 8 * (kg & 1));
  f32x4 acc[8];
  #pragma unroll
  for (int t = 0; t < 8; t++) { acc[t] = f32x4{0, 0, 0, 0}; asm volatile("" : "+v"(acc[t])); }
  bf16x8 A[2][8], Fr[2][8];
  auto ldq = [&](int s, int q, unsigned k, unsigned u) {
    unsigned a = k + (7 - q) * 512;
    u32x2 lo = *(LDS u32x2a*)(size_t)a, hi = *(LDS u32x2a*)(size_t)(a + 8);
    A[s][q] = __builtin_bit_cast(bf16x8, u32x4{lo[0], lo[1], hi[0], hi[1]});
    Fr[s][q] = ld128(u + q * 512);
  };
  auto step = [&](int s, bool nxt, unsigned k, unsigned u) {
    int n = 0;
    #pragma unroll
    for (int d = 0; d < 8; d++)
      #pragma unroll
      for (int q = 0; q <= d; q++) {
        mf16(acc[d], A[s][q], Fr[s][d - q]);
        if ((n & 3) == 1 && (n >> 2) < 8 && nxt) ldq(s ^ 1, n >> 2, k, u);
        n++;
      }
  };
  #pragma unroll
  for (int q = 0; q < 8; q++) ldq(0, q, ka, ua);
  #pragma unroll 1
  for (int it = 0; it < 3; it++) {
    step(0, true, ka - 64, ua - 64);
    ka -= 128; ua -= 128;
    step(1, true, ka, ua);
  }
  step(0, true, ka - 64, ua - 64);
  step(1, false, 0, 0);
  mf_pad();
  const unsigned ue = UB + 2 * (256 + 16 * r + 4 * kg);
  #pragma unroll
  for (int t = 0; t < 8; t++) {
    u32x2 u = *(LDS u32x2*)(size_t)(ue + 512 * t);
    u32x2 o = {pk2(fmaf(bf_lo(u[0]), dsk, acc[t][0]), fmaf(bf_hi(u[0]), dsk, acc[t][1])), pk2(fmaf(bf_lo(u[1]), dsk, acc[t][2]), fmaf(bf_hi(u[1]), dsk, acc[t][3]))};
    __builtin_amdgcn_raw_buffer_store_b64(o, rw, goff + 512 * t + 32 * r + 8 * kg, 0, SC1);
  }
}

DI void layernorm(const unsigned (&xv)[8][5], int tb, rsrc_t rw, int lane) {
      float s8[8];
      const bf16x2 one2 = __builtin_bit_cast(bf16x2, 0x3f803f80u);
      #pragma unroll
      for (int i = 0; i < 8; i++) { float s = 0;
        #pragma unroll
        for (int jj = 0; jj < 5; jj++) s = __builtin_amdgcn_fdot2_f32_bf16(__builtin_bit_cast(bf16x2, xv[i][jj]), one2, s, false);
        s8[i] = s; }
      const float mean = red8(s8, lane) * (1.f / D);
      f32x2 dv[8][5];
      #pragma unroll
      for (int i = 0; i < 8; i++) {
        const float mi = rdl(mean, red8_lane(i));
        f32x2 qq = {0, 0};
        #pragma unroll
        for (int jj = 0; jj < 5; jj++) { dv[i][jj] = f32x2{bf_lo(xv[i][jj]), bf_hi(xv[i][jj])} - mi; qq += dv[i][jj] * dv[i][jj]; }
        s8[i] = qq[0] + qq[1];
      }
      const float var = rbf(red8(s8, lane) * (1.f / D));
      const float den = rbf(__builtin_sqrtf(rbf(var + 1e-5f))), mb = rbf(mean);
      const float ri = 1.f / den, off = (mean - mb) * ri;
      if ((lane & 7) == 0) __builtin_amdgcn_raw_buffer_store_b32(pk2(mb, den), rw, O_STAT + (tb * 8 + ((lane >> 5) | (((lane >> 4) & 1) << 1) | (((lane >> 3) & 1) << 2))) * 4, 0, SC1);
      #pragma unroll
      for (int i = 0; i < 8; i++) {
        const float rr = rdl(ri, red8_lane(i)), oo = rdl(off, red8_lane(i));
        #pragma unroll
        for (int jj = 0; jj < 5; jj++) dv[i][jj] = dv[i][jj] * rr + oo;
      }
      #pragma unroll
      for (int jj = 0; jj < 5; jj++)
        #pragma unroll
        for (int e = 0; e < 2; e++)
          __builtin_amdgcn_raw_buffer_store_b128(u32x4{pk2(dv[0][jj][e], dv[1][jj][e]), pk2(dv[2][jj][e], dv[3][jj][e]), pk2(dv[4][jj][e], dv[5][jj][e]), pk2(dv[6][jj][e], dv[7][jj][e])},
                                                 rw, O_XNB + (tb * 640 + jj * 128 + e * 64 + lane) * 16, 0, SC1);
}

__global__ __launch_bounds__(512, 2) void fused_kernel(const u16* x, const u16* __restrict__ lar, const u16* __restrict__ aim, const u16* __restrict__ cre,
    const u16* __restrict__ cim, const u16* __restrict__ ldt, const u16* dsk, const u16* wg, const u16* wu, const u16* wd,
    u16* out, char* ws) {
  extern __shared__ __attribute__((aligned(16))) char smem[];
  const int b = blockIdx.x, xcd = b & 7, r = b >> 3;
  const int w = __builtin_amdgcn_readfirstlane(threadIdx.x >> 6), lane = threadIdx.x & 63;
  const unsigned l0 = (unsigned)(size_t)(LDS char*)smem;
  asm volatile("" : "+s"(x), "+s"(wg), "+s"(wu), "+s"(wd), "+s"(ws), "+s"(out), "+s"(dsk));
  const rsrc_t rw = __builtin_amdgcn_make_buffer_rsrc(ws, 0, WS_BYTES, 0x00020000);
  ST_INIT(); ST(0);
  const bool gemm = r < 27;
  const int gi = xcd * 27 + r;
  const int id4 = (xcd & 3) * 27 + r, slice = id4 >> 2, panel = (xcd >> 2) * 4 + (id4 & 3);
#define TL 0
#define G2MAP 0
  const int rt = G2MAP ? (xcd >> 1) * 8 + (r >> 2) : xcd * 4 + (r >> 3), cs = G2MAP ? (xcd & 1) * 4 + (r & 3) : r & 7;
  u32x2 e0;
  asm volatile("global_load_dwordx2 %0, %1, %2 sc1" : "=&v"(e0) : "v"(0u), "s"(ws + O_E) : "memory");
  #define GET_TAG(N) do { unsigned lo_, hi_; asm volatile("s_waitcnt vmcnt(" #N ")\n\tv_readfirstlane_b32 %0, %2\n\tv_readfirstlane_b32 %1, %3" : "=&s"(lo_), "=&s"(hi_) : "v"(e0[0]), "v"(e0[1]) : "memory"); \
    tag = (((u64)hi_ << 32) | lo_) * 6364136223846793005ull + 1442695040888963407ull; } while (0)
  u64 tag;

  const int rot = G2MAP ? ((rt & 7) * 7 + (cs & 3) * 14) >> 2 : (cs * 7 + (rt & 3) * 14) >> 2;
  auto kidx = [&](int wv, int i) { int n = wv < 2 ? 14 : 13, v = i + rot; v -= v >= n ? n : 0; v -= v >= n ? n : 0; return wv + 4 * v; };
  auto wd_to_lds = [&](int i0, int i1) {
    #pragma unroll 1
    for (int i = i0; i < i1; i++) {
      const int cw = i / 5;
      const char* src = ws + O_WDT + (size_t)(kidx(cw, 4 + i - cw * 5) * 40 + cs * 5) * 1024;
      wait_vm<50>();
      #pragma unroll
      for (int c = 0; c < 5; c++) glds1(l0 + L_WD + i * 5120 + c * 1024, lane * 16, src + c * 1024);
    }
  };
  const int g = lane >> 4, q = (lane & 15) >> 2, p = lane & 3;

  if (gemm) {
    if (w >= 4) {
      const int j = w - 4, gu = j & 1;
      unsigned xo[2], wo[2];
      #pragma unroll
      for (int e = 0; e < 2; e++) { xo[e] = (lane >> 3) * 1280 + (((lane & 7) ^ ((4 * e + (lane >> 4)) & 7)) * 16); wo[e] = (lane >> 3) * 3456 + (((lane & 7) ^ (2 * (((lane >> 4) & 1) | (e << 1)))) * 16); }
      const char* xb = (const char*)x + (size_t)(panel * 256 + j * 64) * 1280;
      const char* wb = (const char*)(gu ? wu : wg) + (size_t)(32 * (j >> 1)) * 3456 + slice * 128;
      auto issueW = [&](int s, unsigned so) {
        #pragma unroll
        for (int i = 0; i < 4; i++) glds(l0 + so + 32768 + gu * 8192 + (j >> 1) * 4096 + i * 1024, wo[i & 1], wb + (size_t)(s * 64 + 8 * i) * 3456);
      };
      auto issueX = [&](int s, unsigned so) {
        #pragma unroll
        for (int i = 0; i < 8; i++) glds(l0 + so + j * 8192 + i * 1024, xo[i & 1], xb + i * 8 * 1280 + s * 128);
      };
      const unsigned ca = l0 + 32768 + (8 * g + q) * 128 + ((j ^ ((q >> 1) | ((g & 1) << 1))) * 32) + 8 * p;
      const bf16x8 ones = __builtin_bit_cast(bf16x8, u32x4{0x3f803f80u, 0x3f803f80u, 0x3f803f80u, 0x3f803f80u});
      auto csum = [&](unsigned so, f32x4 (&c)[2]) {
        #pragma unroll
        for (int k = 0; k < 4; k++) { unsigned a = ca + so + (k & 1) * 8192 + (k >> 1) * 4096; c[k & 1] = MFMA16(ones, tr2(a, a + 512), c[k & 1]); }
        wait_lgkm0m();
      };
      const unsigned FLA = l0 + L_S;
      issueW(0, 0); issueX(0, 0);
      const char* tp; unsigned tstep;
      {
        const int s0 = ((xcd & 3) * 27) >> 2, ns = (((xcd & 3) * 27 + 26) >> 2) - s0 + 1, n = (r * 4 + j) * 19 + lane;
        const int m = n - 1024, k = min(m / (2 * ns), 63), c = m % (2 * ns);
        tp = n < 1024 ? (const char*)x + (size_t)((xcd >> 2) * 1024 + n) * 1280 : (const char*)(c >= ns ? wu : wg) + (size_t)k * 3456 + (s0 + (c >= ns ? c - ns : c)) * 128;
        tstep = n < 1024 ? 128 : 64 * 3456;
      }
      auto g1_touch = [&](int s) {
        if (lane < (s < 10 ? 19 : 1)) touch(l0 + L_DUMP, tp + (size_t)(s < 10 ? s : 0) * tstep); };
      issueW(1, 2 * SLOT); issueX(1, 2 * SLOT);
      g1_touch(2); g1_touch(3);
      GET_TAG(14); bar();
      issueW(2, SLOT); issueX(2, SLOT);
      f32x4 cl[2] = {};
      bool faok = false;
      auto wd_touch = [&](int i) {
        const int nsh = G2MAP ? 8 : 4, nl = G2MAP ? 23 : 45, n = min((((rt & (nsh - 1)) * 3 + i) * 4 + j) * nl + min(lane, nl - 1), 2159);
        touch1(l0 + L_DUMP, faok ? O_WDT + (n / 40) * 40960 + cs * 5120 + (n % 40) * 128 : O_FA, ws);
      };
      auto get_stats = [&]() { touch1(l0 + L_ST + j * 256, O_STAT + (panel * 256 + j * 64 + lane) * 4, ws); };
      #pragma unroll
      for (int s = 0; s < 8; s++) {
        if (s) {
          wait_vm<16>(); bar();
          const unsigned so = ((s + 2) % 3 == 0 ? 0 : (s + 2) % 3 == 1 ? 2 : 1) * SLOT;
          issueW(s + 2, so); issueX(s + 2, so);
        }
        const int t0 = TL ? 4 + 2 * s : 4 + s, t1 = TL ? 5 + 2 * s : 10;
        if (s == 5 && j == 3) { if (lane < 20) glds1(FLA, O_FA + lane * 16, ws); } else g1_touch(t1);
        g1_touch(t0);
        csum((s % 3 == 0 ? 0 : s % 3 == 1 ? 2 : 1) * SLOT, cl);
      }
      wait_vm<16>(); bar();
      {
        const u32x4 v = *(volatile LDS u32x4*)(size_t)(FLA + (lane < 20 ? lane : 0) * 16);
        faok = __all(v[0] == (unsigned)tag && v[2] == (unsigned)tag && v[1] == (unsigned)(tag >> 32) && v[3] == (unsigned)(tag >> 32));
      }
      get_stats(); wd_touch(0); wd_touch(1); wd_touch(2);
      csum(SLOT, cl);
      wait_vm<6>();
      ST(1);
      bar();
      csum(0, cl);
      *(LDS float*)(size_t)(l0 + L_CS + j * 128 + (lane & 31) * 4) = (g & 1) ? cl[1][0] : cl[0][0];
      if (!faok) { poll(ws, O_FA + (lane < 40 ? lane : 0) * 8, tag); get_stats(); wait_vm<0>(); } else wait_vm<3>();
      wait_lgkm0m();
      lds_put(l0 + L_RDY + 4 * j, 1);
      const int pos = 3 * gi + j;
      const bool hasc = j < 3 && pos < 640;
      const unsigned dvb = *(const GAS u16*)(dsk + pos2ch(hasc ? pos : 0));
      ST(2);
      if (j == 3) {
        wd_to_lds(10, 20);
        lds_wait4<4>(l0 + L_RDY, 1);
        lds_wait4<4>(l0 + L_FREE, 1);
        wd_to_lds(0, 10);
      } else if (hasc) {
        const unsigned KB = l0 + L_CONV + j * CONVB, UB = KB + KXB;
        const char* ub = ws + O_XNB + pos * 16;
        #pragma unroll
        for (int i = 0; i < 4; i++) glds1(UB + 512 + i * 1024, lane * 10240, ub + (size_t)i * 64 * 10240);
        *(LDS u32x2*)(size_t)(UB + lane * 8) = u32x2{0, 0};
        poll(ws, O_FK + pos * 8, tag);
        const u32x4 k01 = __builtin_amdgcn_raw_buffer_load_b128(rw, O_KX + pos * KCB + lane * 16, 0, SC1);
        const u32x2 k2 = __builtin_amdgcn_raw_buffer_load_b64(rw, O_KX + pos * KCB + 1024 + lane * 8, 0, SC1);
        kexpand(k01, k2, KB, lane);
        wait_vm<0>(); wait_lgkm0m();
        lds_wait4<4>(l0 + L_FREE, 1);
        ST(4);
        __builtin_amdgcn_s_setprio(1);
        conv(KB, UB, bf_lo(dvb), rw, O_ST + pos2ch(pos) * 4096, lane);
        ST(5);
        publish(ws, O_FS + pos2ch(pos) * 8, tag, lane);
        ST(3);
      }
    } else {
      if (threadIdx.x < 16) *(volatile LDS unsigned*)(size_t)(l0 + L_FULL + 4 * lane) = 0;
      unsigned wa[4], xa[2], wb[4], xb[2];
      #pragma unroll
      for (int u = 0; u < 4; u++) wa[u] = l0 + 32768 + (8 * g + q) * 128 + ((u ^ ((q >> 1) | ((g & 1) << 1))) * 32) + 8 * p;
      #pragma unroll
      for (int k = 0; k < 2; k++) xa[k] = l0 + (w * 64 + (lane & 15)) * 128 + (((4 * k + g) ^ ((lane >> 1) & 7)) * 16);
      f32x4 acc[8][4];
      #pragma unroll
      for (int f = 0; f < 8; f++)
        #pragma unroll
        for (int t = 0; t < 4; t++) { acc[f][t] = f32x4{0, 0, 0, 0}; asm volatile("" : "+v"(acc[f][t])); }
      bf16x8 X[2][4]; s16x4 W[4][2];
      auto rdx = [&](int k, int t) { X[k][t] = *(LDS bf16x8*)((LDS char*)(size_t)xb[k] + t * 2048); };
      auto rdw = [&](int n, int e) { const int k = n >> 3, f = n & 7; W[n & 3][e] = __builtin_amdgcn_ds_read_tr16_b64_v4i16((LDS s16x4*)((LDS char*)(size_t)wb[f >> 1] + (f & 1) * 8192 + k * 4096 + e * 512)); };
      auto mf = [&](int n, int t) { const int k = n >> 3, f = n & 7, tt = (f & 1) ? 3 - t : t;
        mf16(acc[f][tt], __builtin_bit_cast(bf16x8, __builtin_shufflevector(W[n & 3][0], W[n & 3][1], 0, 1, 2, 3, 4, 5, 6, 7)), X[k][tt]); };
      auto base = [&](unsigned& d, unsigned a, unsigned so) { asm volatile("v_add_u32 %0, %1, %2" : "=v"(d) : "i"(so), "v"(a)); };
      auto body = [&](unsigned son) {
        #pragma unroll
        for (int n = 0; n < 13; n++) {
          mf(n, 0); rdw(n + 3, 0); mf(n, 1); rdw(n + 3, 1); mf(n, 2);
          if (n < 4) rdx(1, n);
          if (n == 4) base(xb[0], xa[0], son);
          if (n == 5) base(xb[1], xa[1], son);
          if (n == 7) base(wb[0], wa[0], son);
          if (n == 9) base(wb[1], wa[1], son);
          if (n == 11) base(wb[2], wa[2], son);
          mf(n, 3);
        }
        base(wb[3], wa[3], son);
      };
      #pragma unroll
      for (int u = 0; u < 4; u++) wb[u] = wa[u];
      xb[0] = xa[0]; xb[1] = xa[1];
      GET_TAG(0);
      wait_lgkm0m();
      bar();
      ST(1);
      #pragma unroll
      for (int t = 0; t < 4; t++) rdx(0, t);
      #pragma unroll
      for (int n = 0; n < 3; n++) { rdw(n, 0); rdw(n, 1); }
      body(2 * SLOT);
      #pragma unroll
      for (int s = 1; s < 10; s++) {
        sb(); wait_lgkm0(); bar();
        mf(13, 0); rdw(0, 0); mf(13, 1); rdw(0, 1); mf(13, 2); rdx(0, 0); mf(13, 3); rdx(0, 1);
        mf(14, 0); rdw(1, 0); mf(14, 1); rdw(1, 1); mf(14, 2); rdx(0, 2); mf(14, 3); rdx(0, 3);
        mf(15, 0); rdw(2, 0); mf(15, 1); rdw(2, 1); mf(15, 2); mf(15, 3);
        body(((s + 1) % 3 == 0 ? 0 : (s + 1) % 3 == 1 ? 2 : 1) * SLOT);
      }
      sb(); wait_lgkm0();
      lds_put(l0 + L_FREE + 4 * w, 1);
      #pragma unroll
      for (int n = 13; n < 16; n++) { mf(n, 0); mf(n, 1); mf(n, 2); mf(n, 3); }
      __builtin_amdgcn_s_setprio(2);
      ST(2);
      lds_wait4<1>(l0 + L_RDY, 1);
      float nm[4], k0[4], k1[4], sq[4];
      #pragma unroll
      for (int t = 0; t < 4; t++) {
        unsigned st = *(LDS unsigned*)(size_t)(l0 + L_ST + (w * 64 + t * 16 + (lane & 15)) * 4);
        float den = bf_hi(st), ri = __builtin_amdgcn_rcpf(den);
        nm[t] = -bf_lo(st); k0[t] = -2.3022082f * ri; k1[t] = -2.3022082f * 0.044715f * ri * ri * ri; sq[t] = den * den;
      }
      #pragma unroll
      for (int f = 0; f < 8; f++) asm volatile("s_nop 0" : "+v"(acc[f][0]), "+v"(acc[f][1]), "+v"(acc[f][2]), "+v"(acc[f][3]));
      #pragma unroll
      for (int u = 0; u < 4; u++) {
        const f32x4 cg = *(LDS f32x4*)(size_t)(l0 + L_CS + u * 128 + g * 16), cu = *(LDS f32x4*)(size_t)(l0 + L_CS + u * 128 + 64 + g * 16);
        unsigned o[4][2];
        #pragma unroll
        for (int t = 0; t < 4; t++)
          #pragma unroll
          for (int e = 0; e < 2; e++) {
            f32x2 gg = f32x2{cg[2 * e], cg[2 * e + 1]} * nm[t] + f32x2{acc[2 * u][t][2 * e], acc[2 * u][t][2 * e + 1]};
            f32x2 uu = f32x2{cu[2 * e], cu[2 * e + 1]} * nm[t] + f32x2{acc[2 * u + 1][t][2 * e], acc[2 * u + 1][t][2 * e + 1]};
            f32x2 a = gg * (gg * gg * k1[t] + k0[t]);
            f32x2 ee = {__builtin_amdgcn_exp2f(a[0]), __builtin_amdgcn_exp2f(a[1])};
            ee = ee * sq[t] + sq[t];
            f32x2 hv = gg * uu * f32x2{__builtin_amdgcn_rcpf(ee[0]), __builtin_amdgcn_rcpf(ee[1])};
            o[t][e] = pk2(hv[0], hv[1]);
          }
        #pragma unroll
        for (int k = 0; k < 2; k++) {
          auto r0 = __builtin_amdgcn_permlane16_swap(o[2 * k][0], o[2 * k + 1][0], false, false), r1 = __builtin_amdgcn_permlane16_swap(o[2 * k][1], o[2 * k + 1][1], false, false);
          __builtin_amdgcn_raw_buffer_store_b128(u32x4{r0[0], r1[0], r0[1], r1[1]}, rw, O_HT + (((panel * 8 + w * 2 + k) * 108 + slice * 4 + u) * 64 + lane) * 16, 0, SC1);
        }
      }
      __builtin_amdgcn_s_setprio(0);
      ST(3);
      publish(ws, O_FH + ((panel * 4 + w) * 32 + slice) * 8, tag, lane);
      ST(4);
    }
  } else {
    const int ai = xcd * 5 + r - 27;
    const unsigned S = l0 + w * AUXW;
    auto arrive = [&]() {
      if (lane == 0) *(volatile LDS u64*)(size_t)(l0 + L_FULL + 64 + w * 8) = tag;
      const u64 v = *(volatile LDS u64*)(size_t)(l0 + L_FULL + 64 + (lane & 7) * 8);
      if (__all(v == tag) && lane == 0) __hip_atomic_store((GAS u64*)(ws + O_FA + ai * 8), tag, RLX);
    };
    if (w < 4) {
      int job[2], tb[2];
      #pragma unroll
      for (int i = 0; i < 2; i++) {
        job[i] = (3 + 4 * i - w) * 40 + ai; tb[i] = (w + 4 * i) * 40 + ai;
        if (job[i] < 270) {
          const char* rb = (const char*)wd + (size_t)(job[i] / 5) * 32 * 1280 + (job[i] % 5) * 256;
          #pragma unroll
          for (int k = 0; k < 8; k++) glds(S + i * 8192 + k * 1024, (lane >> 4) * 1280 + (((lane & 15) ^ (2 * ((lane >> 4) | (k & 2) << 1))) * 16), rb + k * 4 * 1280);
        }
      }
      unsigned xv[2][8][5];
      #pragma unroll
      for (int i = 0; i < 2; i++) {
        const unsigned* xp = (const unsigned*)x + min(tb[i], 255) * 2560 + lane;
        if (i == 0 || tb[1] < 256) {
          #pragma unroll
          for (int k = 0; k < 8; k++)
            #pragma unroll
            for (int jj = 0; jj < 5; jj++) xv[i][k][jj] = xp[k * 320 + jj * 64];
        }
      }
      const KPar P = kpar_load(lar, aim, cre, cim, ldt, pos2ch(480 + ai * 4 + w), lane);
      asm volatile("" :: "v"(xv[0][0][0]) : "memory");
      GET_TAG(63);
      ST(9);
      #pragma unroll
      for (int i = 0; i < 2; i++)
        if (job[i] < 270) {
          #pragma unroll
          for (int c = 0; c < 8; c++) {
            unsigned a = S + i * 8192 + (8 * g + q) * 256 + ((c ^ (q | (g & 1) << 2)) * 32) + 8 * p;
            __builtin_amdgcn_raw_buffer_store_b128(__builtin_bit_cast(u32x4, tr2(a, a + 1024)), rw, O_WDT + (((job[i] / 5) * 40 + (job[i] % 5) * 8 + c) * 64 + lane) * 16, 0, SC1);
          }
        }
      ST(11);
      layernorm(xv[0], tb[0], rw, lane);
      if (tb[1] < 256) layernorm(xv[1], tb[1], rw, lane);
      ST(10);
      wait_vm<0>();
      arrive();
      ST(2);
      kgen(P, S, rw, O_KX + (480 + ai * 4 + w) * KCB, lane);
      publish(ws, O_FK + (480 + ai * 4 + w) * 8, tag, lane);
    } else {
      const int p0 = ai * 4 + w - 4;
      KPar P0 = kpar_load(lar, aim, cre, cim, ldt, pos2ch(p0), lane), P1 = kpar_load(lar, aim, cre, cim, ldt, pos2ch(p0 + 160), lane), P2 = kpar_load(lar, aim, cre, cim, ldt, pos2ch(p0 + 320), lane);
      GET_TAG(0);
      arrive();
      ST(2);
      #pragma unroll 1
      for (int c = 0; c < 3; c++) {
        kgen(P0, S, rw, O_KX + (p0 + 160 * c) * KCB, lane);
        P0 = P1; P1 = P2;
        ST(12 + c);
        publish(ws, O_FK + (p0 + 160 * c) * 8, tag, lane);
      }
    }
    ST(3);
    if (w == 7) lds_put(l0 + L_FLAG, 0);
    wait_lgkm0m();
    bar();
    if (w == 7 || w < 4) poll(ws, O_FA + (lane < 40 ? lane : 0) * 8, tag);
    if (w == 7) wd_to_lds(0, 20);
  }

  if (w == 7) {
    poll(ws, O_FH + (rt * 32 + (lane < 27 ? lane : 0)) * 8, tag);
    lds_put(l0 + L_FLAG, 1);
    ST(5);
    poll(ws, O_FS + (cs * 80 + lane) * 8, O_FS + (cs * 80 + 64 + (lane & 15)) * 8, tag);
    #pragma unroll
    for (int i = 0; i < 10; i++) glds1(l0 + L_S + i * 1024, (lane >> 3) * 4096 + (((lane & 7) ^ (2 * ((lane >> 4) & 3))) * 16), ws + O_ST + (size_t)(cs * 80 + i * 8) * 4096 + rt * 128);
    if (b == 255) {
      #pragma unroll 1
      for (int i = 0; i < 4; i++) { int t = i * 64 + lane; poll(ws, t < 216 ? O_FH + ((t / 27) * 128 + t % 27) * 8 : O_FA + (t - 216) * 8, tag); }
      if (lane == 0) __hip_atomic_store((GAS u64*)(ws + O_E), tag, RLX);
    }
    wait_vm<0>();
    ST(6);
    bar();
  } else if (w >= 4) {
    bar();
  } else {
    const unsigned hb = O_HT + ((rt * 2 * 108 + (lane >> 5)) * 64 + ((lane >> 4) & 1) * 32 + (lane & 15)) * 16;
    const unsigned wb = O_WDT + (cs * 5 * 64 + lane) * 16;
    unsigned ks[14];
    #pragma unroll
    for (int i = 0; i < 14; i++) ks[i] = (i == 13 && w >= 2) ? 0x4000u : kidx(w, i);
    auto ldH = [&](int i, int tt) { return __builtin_bit_cast(bf16x8, __builtin_amdgcn_raw_buffer_load_b128(rw, hb + (tt & 1) * 256, ks[i] * 2048 + (tt >> 1) * 110592, SC1)); };
    auto ldW = [&](int i, int c) { return __builtin_bit_cast(bf16x8, __builtin_amdgcn_raw_buffer_load_b128(rw, wb + c * 1024, ks[i] * 40960, SC1)); };
    bf16x8 W[4][5], H[4][4];
    #pragma unroll
    for (int i = 0; i < 4; i++)
      #pragma unroll
      for (int c = 0; c < 5; c++) W[i][c] = ldW(i, c);
    sb();
    lds_wait(l0 + L_FLAG);
    ST(5);
    constexpr int ord[14] = {0, 4, 1, 5, 2, 6, 3, 7, 9, 8, 10, 11, 12, 13};
    constexpr int set[14] = {0, 0, 1, 1, 2, 2, 3, 3, 0, 0, 1, 2, 3, 0};
    #pragma unroll
    for (int i = 0; i < 3; i++)
      #pragma unroll
      for (int t = 0; t < 4; t++) H[i][t] = ldH(ord[i], t);
    f32x4 acc[4][5];
    u32x2 xr[5];
    const unsigned wl = l0 + L_WD + w * WDW + lane * 16;
    auto kstep = [&](int i) {
      const bool inl = ord[i] >= 4 && ord[i] < 9, nxl = i < 13 && ord[i + 1] >= 4 && ord[i + 1] < 9;
      #pragma unroll
      for (int c = 0; c < 5; c++) {
        #pragma unroll
        for (int t = 0; t < 4; t++) {
          const int tt = (c & 1) ? 3 - t : t;
          if (i == 0) mf16z(acc[tt][c], W[set[i]][c], H[i & 3][tt]); else mf16(acc[tt][c], W[set[i]][c], H[i & 3][tt]);
          if (t == 0 && c < 4 && i + 3 < 14) H[(i + 3) & 3][c] = ldH(ord[i + 3], c);
        }
        if (nxl) W[set[i]][c] = ld128(wl + (ord[i + 1] - 4) * 5120 + c * 1024);
        else if (inl) W[set[i]][c] = ldW(ord[i] + 5, c);
        else if (i == 10) xr[c] = *(const u32x2*)(x + (size_t)(rt * 64 + w * 16 + (lane & 15)) * D + cs * 80 + c * 16 + (lane >> 4) * 4);
      }
    };
    #pragma unroll
    for (int i = 0; i < 13; i++) kstep(i);
    if (w < 2) kstep(13);
    mf_pad();
    sb();
    ST(6);
    #pragma unroll
    for (int t = 0; t < 4; t++)
      #pragma unroll
      for (int c = 0; c < 5; c++) *(LDS f32x4*)(size_t)(wl + (t * 5 + c) * 1024) = acc[t][c];
    wait_lgkm0m();
    bar();
    ST(7);
    #pragma unroll
    for (int c = 0; c < 5; c++) {
      f32x4 o = {0, 0, 0, 0};
      #pragma unroll
      for (int v = 0; v < 4; v++) o += *(LDS f32x4*)(size_t)(l0 + L_WD + v * WDW + (w * 5 + c) * 1024 + lane * 16);
      const int row = c * 16 + 4 * g + q;
      u32x2 sv = __builtin_bit_cast(u32x2, trd(l0 + L_S + row * 128 + ((w ^ ((row >> 1) & 3)) * 32) + 8 * p));
      float sf[4] = {bf_lo(sv[0]), bf_hi(sv[0]), bf_lo(sv[1]), bf_hi(sv[1])}, xf[4] = {bf_lo(xr[c][0]), bf_hi(xr[c][0]), bf_lo(xr[c][1]), bf_hi(xr[c][1])}, y[4];
      #pragma unroll
      for (int e = 0; e < 4; e++) y[e] = fmaf(sf[e], o[e] * __builtin_amdgcn_rcpf(1.f + __builtin_amdgcn_exp2f(-1.4426950408889634f * o[e])), xf[e]);
      __builtin_amdgcn_raw_buffer_store_b64(u32x2{pk2(y[0], y[1]), pk2(y[2], y[3])}, __builtin_amdgcn_make_buffer_rsrc(out, 0, L * D * 2, 0x00020000), ((rt * 64 + w * 16 + (lane & 15)) * D + cs * 80 + c * 16 + g * 4) * 2, 0, 16);
    }
    ST(8);
  }
  ST_DUMP();
}

extern "C" void kernel_launch(void* const* d_in, const int* in_sizes, int n_in, void* d_out, int out_size, void* d_ws, size_t ws_size, hipStream_t stream) {
  (void)hipFuncSetAttribute((const void*)fused_kernel, hipFuncAttributeMaxDynamicSharedMemorySize, LDS_BYTES);
  fused_kernel<<<256, 512, LDS_BYTES, stream>>>((const u16*)d_in[0], (const u16*)d_in[1], (const u16*)d_in[2], (const u16*)d_in[3], (const u16*)d_in[4], (const u16*)d_in[5],
      (const u16*)d_in[6], (const u16*)d_in[8], (const u16*)d_in[9], (const u16*)d_in[10], (u16*)d_out, (char*)d_ws);
}
```

```cpp
#include <hip/hip_runtime.h>
#include <stdint.h>

typedef unsigned short u16;
typedef unsigned long long u64;
typedef __attribute__((ext_vector_type(4))) short s16x4;
typedef __attribute__((ext_vector_type(8))) __bf16 bf16x8;
typedef __attribute__((ext_vector_type(2))) __bf16 bf16x2;
typedef __attribute__((ext_vector_type(2))) float f32x2;
typedef __attribute__((ext_vector_type(4))) float f32x4;
typedef __attribute__((ext_vector_type(16))) float f32x16;
typedef __attribute__((ext_vector_type(2))) unsigned u32x2;
typedef __attribute__((ext_vector_type(4))) unsigned u32x4;
#define LDS __attribute__((address_space(3)))
#define GAS __attribute__((address_space(1)))
#define DI __device__ __forceinline__
#define RLX __ATOMIC_RELAXED, __HIP_MEMORY_SCOPE_AGENT
#define SC1 16

constexpr int L = 2048, D = 640, F = 1728;
constexpr unsigned O_E = 0, O_FA = 1024, O_FK = 2048, O_FH = 8192  , O_FS = 16384;
constexpr unsigned O_STAT = 524288, O_XNB = 1u << 20, O_WDT = 4u << 20, O_KX = 7u << 20, O_HT = 13u << 20, O_ST = 21u << 20, WS_BYTES = 24u << 20;
constexpr unsigned KXB = 8448, KCB = 1536;
constexpr unsigned SLOT = 49152;
constexpr unsigned L_WD = 0, WDW = 25600;
constexpr unsigned L_CONV = 102400, CONVB = 13056;
constexpr unsigned L_S = 147456;
constexpr unsigned L_CS = 159744;
constexpr unsigned L_ST = 160256;
constexpr unsigned L_FULL = 161280, L_FREE = L_FULL + 16, L_RDY = L_FULL + 32, L_FLAG = L_FULL + 48, L_DUMP = L_FULL + 256;
constexpr unsigned LDS_BYTES = 163840;
constexpr unsigned AUXW = 20480;
#define SPL 20000
constexpr int SPIN_LIMIT = SPL;

#define ST(i) do {} while (0)
#define ST_INIT() do {} while (0)
#define ST_DUMP() do {} while (0)

DI float bf_lo(unsigned v) { return __uint_as_float(v << 16); }
DI float bf_hi(unsigned v) { return __uint_as_float(v & 0xffff0000u); }
DI unsigned pk2(float a, float b) { return __builtin_bit_cast(unsigned, __builtin_convertvector(f32x2{a, b}, bf16x2)); }
DI float rbf(float a) { return bf_lo(pk2(a, a) & 0xffffu); }

DI void glds(unsigned lds, unsigned voff, const char* sbase) {
  asm volatile("s_mov_b32 m0, %0\n\ts_nop 0\n\tglobal_load_lds_dwordx4 %1, %2" :: "s"(lds), "v"(voff), "s"(sbase) : "memory");
}
DI void glds1(unsigned lds, unsigned voff, const char* sbase) {
  asm volatile("s_mov_b32 m0, %0\n\ts_nop 0\n\tglobal_load_lds_dwordx4 %1, %2 sc1" :: "s"(lds), "v"(voff), "s"(sbase) : "memory");
}
DI void touch(unsigned lds, const char* p) { asm volatile("s_mov_b32 m0, %0\n\ts_nop 0\n\tglobal_load_lds_dword %1, off" :: "s"(lds), "v"(p) : "memory"); }
DI void touch1(unsigned lds, unsigned voff, const char* sbase) { asm volatile("s_mov_b32 m0, %0\n\ts_nop 0\n\tglobal_load_lds_dword %1, %2 sc1" :: "s"(lds), "v"(voff), "s"(sbase) : "memory"); }
template<int N> DI void wait_vm() { asm volatile("s_waitcnt vmcnt(%0)" :: "i"(N) : "memory"); }
template<int B> DI void wait_vmx(int n) {
  if (n == 0) wait_vm<B>();
  else if (n < 3) { if (n == 1) wait_vm<B + 1>(); else wait_vm<B + 2>(); }
  else if (n < 5) { if (n == 3) wait_vm<B + 3>(); else wait_vm<B + 4>(); }
  else if (n == 5) wait_vm<B + 5>(); else wait_vm<B + 6>();
}
DI void sb() { __builtin_amdgcn_sched_barrier(0); }
DI void bar() { sb(); __builtin_amdgcn_s_barrier(); sb(); }
DI void wait_lgkm0() { __builtin_amdgcn_s_waitcnt(0xC07F); }
DI void wait_lgkm0m() { asm volatile("s_waitcnt lgkmcnt(0)" ::: "memory"); }
DI s16x4 trd(unsigned a) { return __builtin_amdgcn_ds_read_tr16_b64_v4i16((LDS s16x4*)(size_t)a); }
DI bf16x8 tr2(unsigned a, unsigned b) { return __builtin_bit_cast(bf16x8, __builtin_shufflevector(trd(a), trd(b), 0, 1, 2, 3, 4, 5, 6, 7)); }
DI bf16x8 ld128(unsigned a) { return *(LDS bf16x8*)(size_t)a; }
#define MFMA32(a, b, c) __builtin_amdgcn_mfma_f32_32x32x16_bf16(a, b, c, 0, 0, 0)
#define MFMA16(a, b, c) __builtin_amdgcn_mfma_f32_16x16x32_bf16(a, b, c, 0, 0, 0)
#define SGB(m, n) __builtin_amdgcn_sched_group_barrier(m, n, 0)
typedef __amdgpu_buffer_rsrc_t rsrc_t;
DI void mf16(f32x4& c, const bf16x8& a, const bf16x8& b) { asm volatile("v_mfma_f32_16x16x32_bf16 %0, %1, %2, %0" : "+v"(c) : "v"(a), "v"(b)); }
DI void mf16z(f32x4& c, const bf16x8& a, const bf16x8& b) { asm volatile("v_mfma_f32_16x16x32_bf16 %0, %1, %2, 0" : "=&v"(c) : "v"(a), "v"(b)); }
DI void mf_pad() { asm volatile("s_nop 7\n\ts_nop 1" ::: "memory"); }
typedef u32x2 __attribute__((aligned(4))) u32x2a;

DI void poll(const char* ws, unsigned off, unsigned off2, u64 tag) {
  #pragma unroll 1
  for (int it = 0; it < SPIN_LIMIT; it++) {
    const u64 v = __hip_atomic_load((const GAS u64*)(ws + off), RLX), v2 = __hip_atomic_load((const GAS u64*)(ws + off2), RLX);
    if (__all(v == tag && v2 == tag)) break;
    __builtin_amdgcn_s_sleep(2);
  }
  asm volatile("" ::: "memory");
}
DI void poll(const char* ws, unsigned off, u64 tag) {
  #pragma unroll 1
  for (int it = 0; it < SPIN_LIMIT; it++) {
    if (__all(__hip_atomic_load((const GAS u64*)(ws + off), RLX) == tag)) break;
    __builtin_amdgcn_s_sleep(2);
  }
  asm volatile("" ::: "memory");
}
DI void publish(char* ws, unsigned off, u64 tag, int lane) {
  asm volatile("s_waitcnt vmcnt(0)" ::: "memory");
  if (lane == 0) __hip_atomic_store((GAS u64*)(ws + off), tag, RLX);
}
DI void lds_wait(unsigned a) {
  #pragma unroll 1
  for (int it = 0; it < 16 * SPIN_LIMIT; it++) {
    if (__builtin_amdgcn_readfirstlane(*(volatile LDS unsigned*)(size_t)a)) break;
    __builtin_amdgcn_s_sleep(1);
  }
  asm volatile("" ::: "memory");
}
DI unsigned min4(u32x4 v) { return __builtin_amdgcn_readfirstlane(min(min(v[0], v[1]), min(v[2], v[3]))); }
DI unsigned lds_min4(unsigned a) { return min4(*(volatile LDS u32x4*)(size_t)a); }
template<int SL> DI void lds_wait4(unsigned a, unsigned need) {
  #pragma unroll 1
  for (int it = 0; it < 16 * SPIN_LIMIT && lds_min4(a) < need; it++) __builtin_amdgcn_s_sleep(SL);
  asm volatile("" ::: "memory");
}
DI void lds_put(unsigned a, unsigned v) { if ((threadIdx.x & 63) == 0) *(volatile LDS unsigned*)(size_t)a = v; }
DI int pos2ch(int pos) { return (pos & ~127) + 2 * (pos & 63) + ((pos >> 6) & 1); }

DI float red8(const float (&a)[8], int lane) {
  float b[4], c[2];
  #pragma unroll
  for (int i = 0; i < 4; i++) { auto r = __builtin_amdgcn_permlane32_swap(__float_as_uint(a[2 * i]), __float_as_uint(a[2 * i + 1]), false, false); b[i] = __uint_as_float(r[0]) + __uint_as_float(r[1]); }
  #pragma unroll
  for (int i = 0; i < 2; i++) { auto r = __builtin_amdgcn_permlane16_swap(__float_as_uint(b[2 * i]), __float_as_uint(b[2 * i + 1]), false, false); c[i] = __uint_as_float(r[0]) + __uint_as_float(r[1]); }
  bool up = lane & 8;
  float keep = up ? c[1] : c[0], send = up ? c[0] : c[1];
  float d = keep + __uint_as_float(__builtin_amdgcn_update_dpp(0, __float_as_uint(send), 0x128, 0xf, 0xf, true));
  d += __uint_as_float(__builtin_amdgcn_update_dpp(0, __float_as_uint(d), 0x141, 0xf, 0xf, true));
  d += __uint_as_float(__builtin_amdgcn_update_dpp(0, __float_as_uint(d), 0xB1, 0xf, 0xf, true));
  d += __uint_as_float(__builtin_amdgcn_update_dpp(0, __float_as_uint(d), 0x4E, 0xf, 0xf, true));
  return d;
}
DI constexpr int red8_lane(int i) { return ((i & 1) << 5) | (((i >> 1) & 1) << 4) | (((i >> 2) & 1) << 3); }
DI float rdl(float v, int l) { return __uint_as_float(__builtin_amdgcn_readlane(__float_as_uint(v), l)); }

DI f32x2 cmul(f32x2 a, f32x2 b) {
  f32x2 r;
  asm("v_pk_mul_f32 %0, %1, %2 op_sel:[1,1] op_sel_hi:[1,0] neg_lo:[0,1]\n\tv_pk_fma_f32 %0, %1, %2, %0 op_sel_hi:[0,1,1]" : "=&v"(r) : "v"(a), "v"(b));
  return r;
}
DI f32x2 zpow(float al, float be, float p) {
  float e = __builtin_amdgcn_exp2f(al * p), f = __builtin_amdgcn_fractf(be * p);
  return f32x2{e * __builtin_amdgcn_cosf(f), e * __builtin_amdgcn_sinf(f)};
}

typedef __attribute__((ext_vector_type(2))) _Float16 h2;
typedef __attribute__((ext_vector_type(8))) _Float16 f16x8;
DI h2 cmulh(h2 a, h2 b) { h2 t = h2{a[1], a[1]} * h2{-b[1], b[0]}; return __builtin_elementwise_fma(h2{a[0], a[0]}, b, t); }
DI h2 toh(f32x2 a) { return __builtin_convertvector(a, h2); }
DI h2 tohc(f32x2 a) { h2 r; asm("v_cvt_pk_f16_f32 %0, %1, -%2" : "=v"(r) : "v"(a[0]), "v"(a[1])); return r; }
template<int C> DI float maxd(float v) { return fmaxf(v, __uint_as_float(__builtin_amdgcn_update_dpp(0, __float_as_uint(v), C, 0xf, 0xf, true))); }

struct KPar { unsigned lar, aim, cre, cim; unsigned ldt; };
DI KPar kpar_load(const u16* lar, const u16* aim, const u16* cre, const u16* cim, const u16* ldt, int ch, int lane) {
  KPar p; int o = ch * 64 + lane;
  p.lar = ((const GAS unsigned*)lar)[o]; p.aim = ((const GAS unsigned*)aim)[o]; p.cre = ((const GAS unsigned*)cre)[o]; p.cim = ((const GAS unsigned*)cim)[o];
  p.ldt = ((const GAS u16*)ldt)[ch];
  return p;
}
DI void kgen(const KPar& P, unsigned S, rsrc_t rw, unsigned goff, int lane) {
  const float LOG2E = 1.4426950408889634f;
  const float dt = rbf(__builtin_amdgcn_exp2f(bf_lo(P.ldt) * LOG2E));
  f32x2 C[2] = {f32x2{bf_lo(P.cre), bf_lo(P.cim)}, f32x2{bf_hi(P.cre), bf_hi(P.cim)}};
  float mx = fmaxf(fmaxf(fabsf(C[0][0]), fabsf(C[0][1])), fmaxf(fabsf(C[1][0]), fabsf(C[1][1])));
  mx = maxd<0xB1>(mx); mx = maxd<0x4E>(mx); mx = maxd<0x141>(mx); mx = maxd<0x140>(mx);
  mx = fmaxf(fmaxf(rdl(mx, 0), rdl(mx, 16)), fmaxf(rdl(mx, 32), rdl(mx, 48)));
  const int ex = __builtin_amdgcn_frexp_expf(mx);
  const float cs = __builtin_amdgcn_ldexpf(1.f, -ex), ks = __builtin_amdgcn_ldexpf(2.f * dt, ex);
  h2 T[2][8], Pw[2][4], Q[2][4], U[2][17];
  #pragma unroll
  for (int m = 0; m < 2; m++) {
    float la = m ? bf_hi(P.lar) : bf_lo(P.lar), ai = m ? bf_hi(P.aim) : bf_lo(P.aim);
    float al = -rbf(__builtin_amdgcn_exp2f(la * LOG2E)) * dt * LOG2E, be = ai * dt * 0.15915494309189535f;
    f32x2 z1 = zpow(al, be, 1.f), u1 = zpow(al, be, 128.f);
    f32x2 z2 = cmul(z1, z1), z3 = cmul(z2, z1), z4 = cmul(z2, z2), z8 = cmul(z4, z4), z16 = cmul(z8, z8), z24 = cmul(z16, z8), z32 = cmul(z16, z16);
    f32x2 q0 = C[m] * cs, q1 = cmul(q0, z32), q2 = cmul(q1, z32), q3 = cmul(q2, z32);
    f32x2 u2 = cmul(u1, u1), u3 = cmul(u2, u1), u4 = cmul(u2, u2), u8 = cmul(u4, u4), u12 = cmul(u8, u4), u16 = cmul(u8, u8);
    T[m][0] = h2{1, 0}; T[m][1] = toh(z1); T[m][2] = toh(z2); T[m][3] = toh(z3); T[m][4] = toh(z4);
    Pw[m][1] = tohc(z8); Pw[m][2] = tohc(z16); Pw[m][3] = tohc(z24);
    Q[m][0] = tohc(q0); Q[m][1] = tohc(q1); Q[m][2] = tohc(q2); Q[m][3] = tohc(q3);
    U[m][1] = toh(u1); U[m][2] = toh(u2); U[m][3] = toh(u3); U[m][4] = toh(u4); U[m][8] = toh(u8); U[m][12] = toh(u12); U[m][16] = toh(u16);
  }
  const unsigned wa = S + lane * 8, ra = S + (lane & 15) * 528 + (lane >> 4) * 16;
  auto put = [&](int row, h2 v0, h2 v1) { *(LDS u32x2*)((LDS char*)(size_t)wa + row * 528) = u32x2{__builtin_bit_cast(unsigned, v0), __builtin_bit_cast(unsigned, v1)}; };
  auto frag = [&](int k) { return *(LDS f16x8*)((LDS char*)(size_t)ra + k * 64); };
  f16x8 fine[8];
  #pragma unroll
  for (int b = 0; b < 16; b++) { if (b & 3) put(b, cmulh(Q[0][b >> 2], Pw[0][b & 3]), cmulh(Q[1][b >> 2], Pw[1][b & 3])); else put(b, Q[0][b >> 2], Q[1][b >> 2]); }
  #pragma unroll
  for (int k = 0; k < 8; k++) fine[k] = frag(k);
  f32x4 acc[3];
  auto tile = [&](int t) {
    f32x4 a = {0, 0, 0, 0}, a2 = {0, 0, 0, 0};
    #pragma unroll
    for (int k = 0; k < 8; k += 2) { a = __builtin_amdgcn_mfma_f32_16x16x32_f16(frag(k), fine[k], a, 0, 0, 0); a2 = __builtin_amdgcn_mfma_f32_16x16x32_f16(frag(k + 1), fine[k + 1], a2, 0, 0, 0); }
    acc[t] = (a + a2) * ks;
  };
  #pragma unroll
  for (int e = 5; e < 8; e++) { T[0][e] = cmulh(T[0][4], T[0][e - 4]); T[1][e] = cmulh(T[1][4], T[1][e - 4]); }
  #pragma unroll
  for (int e = 0; e < 8; e++) put(e, T[0][e], T[1][e]);
  put(8, U[0][1], U[1][1]);
  #pragma unroll
  for (int e = 1; e < 8; e++) put(8 + e, cmulh(T[0][e], U[0][1]), cmulh(T[1][e], U[1][1]));
  tile(0);
  #pragma unroll
  for (int a = 2; a < 4; a++) {
    put(4 * (a - 2), U[0][a], U[1][a]);
    #pragma unroll
    for (int e = 1; e < 4; e++) put(4 * (a - 2) + e, cmulh(T[0][2 * e], U[0][a]), cmulh(T[1][2 * e], U[1][a]));
  }
  #pragma unroll
  for (int a = 4; a < 8; a++) {
    if (a > 4) { U[0][a] = cmulh(U[0][4], U[0][a - 4]); U[1][a] = cmulh(U[1][4], U[1][a - 4]); }
    put(8 + 2 * (a - 4), U[0][a], U[1][a]);
    put(9 + 2 * (a - 4), cmulh(T[0][4], U[0][a]), cmulh(T[1][4], U[1][a]));
  }
  tile(1);
  #pragma unroll
  for (int a = 8; a <= 16; a++) { if (a & 3) put(a - 8, cmulh(U[0][a & ~3], U[0][a & 3]), cmulh(U[1][a & ~3], U[1][a & 3])); else put(a - 8, U[0][a], U[1][a]); }
  tile(2);
  __builtin_amdgcn_raw_buffer_store_b128(u32x4{pk2(acc[0][0], acc[0][1]), pk2(acc[0][2], acc[0][3]), pk2(acc[1][0], acc[1][1]), pk2(acc[1][2], acc[1][3])}, rw, goff + lane * 16, 0, SC1);
  __builtin_amdgcn_raw_buffer_store_b64(u32x2{pk2(acc[2][0], acc[2][1]), pk2(acc[2][2], acc[2][3])}, rw, goff + 1024 + lane * 8, 0, SC1);
}
DI unsigned prm(unsigned hi, unsigned lo, unsigned sel) { return __builtin_amdgcn_perm(hi, lo, sel); }
DI void kexpand(u32x4 k01, u32x2 k2, unsigned S, int lane) {
  const int b = lane & 15, g = lane >> 4;
  const unsigned TR = S + 4608;
  const unsigned LL = 0x01000100u, HH = 0x03020302u;
  unsigned a3 = g < 2 ? S + 8 + 2 * (2044 - (1024 + 512 * g + 384 + 8 * b)) : TR;
  unsigned a4 = (g < 2 && !(b & 1)) ? a3 - 2 : TR;
  unsigned v3[4];
  #pragma unroll
  for (int j = 0; j < 4; j++) { v3[j] = prm(0, k2[j >> 1], (j & 1) ? HH : LL); *(LDS u32x4*)((LDS char*)(size_t)a3 + 256 * (3 - j)) = u32x4{v3[j], v3[j], v3[j], v3[j]}; }
  *(LDS u32x4*)(size_t)(lane == 32 ? S : TR) = u32x4{v3[0], v3[0], v3[0], v3[0]};
  #pragma unroll
  for (int j = 0; j < 4; j++) *(volatile LDS u16*)((LDS char*)(size_t)a4 + 256 * (3 - j)) = (u16)v3[j];
  unsigned a2 = g >= 2 ? S + 8 + 2 * (2042 - (512 + 256 * (g - 2) + 128 + 8 * b)) : TR;
  const unsigned l0_ = prm(0, k01[2], LL), h0_ = prm(0, k01[2], HH), l1_ = prm(0, k01[3], LL), h1_ = prm(0, k01[3], HH);
  #pragma unroll
  for (int h = 0; h < 2; h++) {
    volatile LDS unsigned* q = (volatile LDS unsigned*)((LDS char*)(size_t)a2 + 256 * (1 - h));
    q[0] = h ? h1_ : h0_; q[1] = prm(0, k01[2 + h], 0x01000302u); q[2] = h ? l1_ : l0_; q[3] = h ? l1_ : l0_;
  }
  LDS char* a1 = (LDS char*)(size_t)(g < 2 ? S + 8 + 2 * (2041 - (256 + 128 * g + 8 * b)) : TR + 2);
  *(volatile LDS u16*)a1 = (u16)h1_;
  *(volatile LDS unsigned*)(a1 + 2) = l1_;
  *(volatile LDS unsigned*)(a1 + 6) = prm(k01[2], k01[3], 0x07060100u);
  *(volatile LDS unsigned*)(a1 + 10) = l0_;
  *(volatile LDS u16*)(a1 + 14) = (u16)l0_;
  unsigned a0 = S + 8 + 2 * (2044 - (128 * (g >> 1) + 4 * (g & 1) + 8 * b));
  *(volatile LDS u32x2*)(size_t)a0 = u32x2{prm(0, k01[1], 0x01000302u), prm(0, k01[0], 0x01000302u)};
  *(volatile LDS u32x2*)(size_t)(lane < 6 ? S + 8 + 4096 + 8 * lane : TR) = u32x2{0, 0};
  asm volatile("" ::: "memory");
  u32x2 x[5], y[5]; unsigned z[5];
  #pragma unroll
  for (int i = 0; i < 5; i++) {
    LDS char* a = (LDS char*)(size_t)(S + 8 + 16 * (i < 4 ? lane : (lane & 1))) + 1024 * i;
    x[i] = *(LDS u32x2*)a; y[i] = *(LDS u32x2*)(a + 8); z[i] = *(LDS unsigned*)(a + 16);
  }
  asm volatile("" ::: "memory");
  #pragma unroll
  for (int i = 0; i < 5; i++) {
    LDS char* a = (LDS char*)(size_t)(S + 4168 + 16 * (i < 4 ? lane : (lane & 1))) + 1024 * i;
    if (i < 4 || lane < 2) {
      *(LDS u32x2*)a = u32x2{__builtin_amdgcn_alignbit(x[i][1], x[i][0], 16), __builtin_amdgcn_alignbit(y[i][0], x[i][1], 16)};
      *(LDS u32x2*)(a + 8) = u32x2{__builtin_amdgcn_alignbit(y[i][1], y[i][0], 16), __builtin_amdgcn_alignbit(z[i], y[i][1], 16)};
    }
  }
  asm volatile("" ::: "memory");
}

DI void conv(unsigned KB, unsigned UB, float dsk, rsrc_t rw, unsigned goff, int lane) {
  const int r = lane & 15, kg = lane >> 4;
  const int n0 = 2047 - (16 * 112 + 16 * (kg >> 1) + r - 8 * (kg & 1));
  unsigned ka = KB + 8 + ((n0 & 1) ? 4160 + 2 * (n0 - 1) : 2 * n0);
  unsigned ua = UB + 2 * (256 + 16 * r - 16 * (kg >> 1) + 8 * (kg & 1));
  f32x4 acc[8];
  #pragma unroll
  for (int t = 0; t < 8; t++) { acc[t] = f32x4{0, 0, 0, 0}; asm volatile("" : "+v"(acc[t])); }
  bf16x8 A[2][8], Fr[2][8];
  auto ldq = [&](int s, int q, unsigned k, unsigned u) {
    unsigned a = k + (7 - q) * 512;
    u32x2 lo = *(LDS u32x2a*)(size_t)a, hi = *(LDS u32x2a*)(size_t)(a + 8);
    A[s][q] = __builtin_bit_cast(bf16x8, u32x4{lo[0], lo[1], hi[0], hi[1]});
    Fr[s][q] = ld128(u + q * 512);
  };
  auto step = [&](int s, bool nxt, unsigned k, unsigned u) {
    int n = 0;
    #pragma unroll
    for (int d = 0; d < 8; d++)
      #pragma unroll
      for (int q = 0; q <= d; q++) {
        mf16(acc[d], A[s][q], Fr[s][d - q]);
        if ((n & 3) == 1 && (n >> 2) < 8 && nxt) ldq(s ^ 1, n >> 2, k, u);
        n++;
      }
  };
  #pragma unroll
  for (int q = 0; q < 8; q++) ldq(0, q, ka, ua);
  #pragma unroll 1
  for (int it = 0; it < 3; it++) {
    step(0, true, ka - 64, ua - 64);
    ka -= 128; ua -= 128;
    step(1, true, ka, ua);
  }
  step(0, true, ka - 64, ua - 64);
  step(1, false, 0, 0);
  mf_pad();
  const unsigned ue = UB + 2 * (256 + 16 * r + 4 * kg);
  #pragma unroll
  for (int t = 0; t < 8; t++) {
    u32x2 u = *(LDS u32x2*)(size_t)(ue + 512 * t);
    u32x2 o = {pk2(fmaf(bf_lo(u[0]), dsk, acc[t][0]), fmaf(bf_hi(u[0]), dsk, acc[t][1])), pk2(fmaf(bf_lo(u[1]), dsk, acc[t][2]), fmaf(bf_hi(u[1]), dsk, acc[t][3]))};
    __builtin_amdgcn_raw_buffer_store_b64(o, rw, goff + 512 * t + 32 * r + 8 * kg, 0, SC1);
  }
}

DI void layernorm(const unsigned (&xv)[8][5], int tb, rsrc_t rw, int lane) {
      float s8[8];
      const bf16x2 one2 = __builtin_bit_cast(bf16x2, 0x3f803f80u);
      #pragma unroll
      for (int i = 0; i < 8; i++) { float s = 0;
        #pragma unroll
        for (int jj = 0; jj < 5; jj++) s = __builtin_amdgcn_fdot2_f32_bf16(__builtin_bit_cast(bf16x2, xv[i][jj]), one2, s, false);
        s8[i] = s; }
      const float mean = red8(s8, lane) * (1.f / D);
      f32x2 dv[8][5];
      #pragma unroll
      for (int i = 0; i < 8; i++) {
        const float mi = rdl(mean, red8_lane(i));
        f32x2 qq = {0, 0};
        #pragma unroll
        for (int jj = 0; jj < 5; jj++) { dv[i][jj] = f32x2{bf_lo(xv[i][jj]), bf_hi(xv[i][jj])} - mi; qq += dv[i][jj] * dv[i][jj]; }
        s8[i] = qq[0] + qq[1];
      }
      const float var = rbf(red8(s8, lane) * (1.f / D));
      const float den = rbf(__builtin_sqrtf(rbf(var + 1e-5f))), mb = rbf(mean);
      const float ri = 1.f / den, off = (mean - mb) * ri;
      if ((lane & 7) == 0) __builtin_amdgcn_raw_buffer_store_b32(pk2(mb, den), rw, O_STAT + (tb * 8 + ((lane >> 5) | (((lane >> 4) & 1) << 1) | (((lane >> 3) & 1) << 2))) * 4, 0, SC1);
      #pragma unroll
      for (int i = 0; i < 8; i++) {
        const float rr = rdl(ri, red8_lane(i)), oo = rdl(off, red8_lane(i));
        #pragma unroll
        for (int jj = 0; jj < 5; jj++) dv[i][jj] = dv[i][jj] * rr + oo;
      }
      #pragma unroll
      for (int jj = 0; jj < 5; jj++)
        #pragma unroll
        for (int e = 0; e < 2; e++)
          __builtin_amdgcn_raw_buffer_store_b128(u32x4{pk2(dv[0][jj][e], dv[1][jj][e]), pk2(dv[2][jj][e], dv[3][jj][e]), pk2(dv[4][jj][e], dv[5][jj][e]), pk2(dv[6][jj][e], dv[7][jj][e])},
                                                 rw, O_XNB + (tb * 640 + jj * 128 + e * 64 + lane) * 16, 0, SC1);
}

__global__ __launch_bounds__(512, 2) void fused_kernel(const u16* x, const u16* __restrict__ lar, const u16* __restrict__ aim, const u16* __restrict__ cre,
    const u16* __restrict__ cim, const u16* __restrict__ ldt, const u16* dsk, const u16* wg, const u16* wu, const u16* wd,
    u16* out, char* ws) {
  extern __shared__ __attribute__((aligned(16))) char smem[];
  const int b = blockIdx.x, xcd = b & 7, r = b >> 3;
  const int w = __builtin_amdgcn_readfirstlane(threadIdx.x >> 6), lane = threadIdx.x & 63;
  const unsigned l0 = (unsigned)(size_t)(LDS char*)smem;
  asm volatile("" : "+s"(x), "+s"(wg), "+s"(wu), "+s"(wd), "+s"(ws), "+s"(out), "+s"(dsk));
  const rsrc_t rw = __builtin_amdgcn_make_buffer_rsrc(ws, 0, WS_BYTES, 0x00020000);
  ST_INIT(); ST(0);
  const bool gemm = r < 27;
  const int gi = xcd * 27 + r;
  const int id4 = (xcd & 3) * 27 + r, slice = id4 >> 2, panel = (xcd >> 2) * 4 + (id4 & 3);
#define TL 0
#define G2MAP 0
  const int rt = G2MAP ? (xcd >> 1) * 8 + (r >> 2) : xcd * 4 + (r >> 3), cs = G2MAP ? (xcd & 1) * 4 + (r & 3) : r & 7;
  u32x2 e0;
  asm volatile("global_load_dwordx2 %0, %1, %2 sc1" : "=&v"(e0) : "v"(0u), "s"(ws + O_E) : "memory");
  #define GET_TAG(N) do { unsigned lo_, hi_; asm volatile("s_waitcnt vmcnt(" #N ")\n\tv_readfirstlane_b32 %0, %2\n\tv_readfirstlane_b32 %1, %3" : "=&s"(lo_), "=&s"(hi_) : "v"(e0[0]), "v"(e0[1]) : "memory"); \
    tag = (((u64)hi_ << 32) | lo_) * 6364136223846793005ull + 1442695040888963407ull; } while (0)
  u64 tag;

  const int rot = G2MAP ? ((rt & 7) * 7 + (cs & 3) * 14) >> 2 : (cs * 7 + (rt & 3) * 14) >> 2;
  auto kidx = [&](int wv, int i) { int n = wv < 2 ? 14 : 13, v = i + rot; v -= v >= n ? n : 0; v -= v >= n ? n : 0; return wv + 4 * v; };
  auto wd_to_lds = [&](int i0, int i1) {
    #pragma unroll 1
    for (int i = i0; i < i1; i++) {
      const int cw = i / 5;
      const char* src = ws + O_WDT + (size_t)(kidx(cw, 4 + i - cw * 5) * 40 + cs * 5) * 1024;
      wait_vm<50>();
      #pragma unroll
      for (int c = 0; c < 5; c++) glds1(l0 + L_WD + i * 5120 + c * 1024, lane * 16, src + c * 1024);
    }
  };
  const int g = lane >> 4, q = (lane & 15) >> 2, p = lane & 3;

  if (gemm) {
    if (w >= 4) {
      const int j = w - 4, gu = j & 1;
      unsigned xo[2], wo[2];
      #pragma unroll
      for (int e = 0; e < 2; e++) { xo[e] = (lane >> 3) * 1280 + (((lane & 7) ^ ((4 * e + (lane >> 4)) & 7)) * 16); wo[e] = (lane >> 3) * 3456 + (((lane & 7) ^ (2 * (((lane >> 4) & 1) | (e << 1)))) * 16); }
      const char* xb = (const char*)x + (size_t)(panel * 256 + j * 64) * 1280;
      const char* wb = (const char*)(gu ? wu : wg) + (size_t)(32 * (j >> 1)) * 3456 + slice * 128;
      auto issueW = [&](int s, unsigned so) {
        #pragma unroll
        for (int i = 0; i < 4; i++) glds(l0 + so + 32768 + gu * 8192 + (j >> 1) * 4096 + i * 1024, wo[i & 1], wb + (size_t)(s * 64 + 8 * i) * 3456);
      };
      auto issueX = [&](int s, unsigned so) {
        #pragma unroll
        for (int i = 0; i < 8; i++) glds(l0 + so + j * 8192 + i * 1024, xo[i & 1], xb + i * 8 * 1280 + s * 128);
      };
      const unsigned ca = l0 + 32768 + (8 * g + q) * 128 + ((j ^ ((q >> 1) | ((g & 1) << 1))) * 32) + 8 * p;
      const bf16x8 ones = __builtin_bit_cast(bf16x8, u32x4{0x3f803f80u, 0x3f803f80u, 0x3f803f80u, 0x3f803f80u});
      auto csum = [&](unsigned so, f32x4 (&c)[2]) {
        #pragma unroll
        for (int k = 0; k < 4; k++) { unsigned a = ca + so + (k & 1) * 8192 + (k >> 1) * 4096; c[k & 1] = MFMA16(ones, tr2(a, a + 512), c[k & 1]); }
        wait_lgkm0m();
      };
      const unsigned FLA = l0 + L_S;
      issueW(0, 0); issueX(0, 0);
      const char* tp; unsigned tstep;
      {
        const int s0 = ((xcd & 3) * 27) >> 2, ns = (((xcd & 3) * 27 + 26) >> 2) - s0 + 1, n = (r * 4 + j) * 19 + lane;
        const int m = n - 1024, k = min(m / (2 * ns), 63), c = m % (2 * ns);
        tp = n < 1024 ? (const char*)x + (size_t)((xcd >> 2) * 1024 + n) * 1280 : (const char*)(c >= ns ? wu : wg) + (size_t)k * 3456 + (s0 + (c >= ns ? c - ns : c)) * 128;
        tstep = n < 1024 ? 128 : 64 * 3456;
      }
      auto g1_touch = [&](int s) {
        if (lane < (s < 10 ? 19 : 1)) touch(l0 + L_DUMP, tp + (size_t)(s < 10 ? s : 0) * tstep); };
      issueW(1, 2 * SLOT); issueX(1, 2 * SLOT);
      g1_touch(2); g1_touch(3);
      GET_TAG(14); bar();
      issueW(2, SLOT); issueX(2, SLOT);
      f32x4 cl[2] = {};
      bool faok = false;
      auto wd_touch = [&](int i) {
        const int nsh = G2MAP ? 8 : 4, nl = G2MAP ? 23 : 45, n = min((((rt & (nsh - 1)) * 3 + i) * 4 + j) * nl + min(lane, nl - 1), 2159);
        touch1(l0 + L_DUMP, faok ? O_WDT + (n / 40) * 40960 + cs * 5120 + (n % 40) * 128 : O_FA, ws);
      };
      auto get_stats = [&]() { touch1(l0 + L_ST + j * 256, O_STAT + (panel * 256 + j * 64 + lane) * 4, ws); };
      #pragma unroll
      for (int s = 0; s < 8; s++) {
        if (s) {
          wait_vm<16>(); bar();
          const unsigned so = ((s + 2) % 3 == 0 ? 0 : (s + 2) % 3 == 1 ? 2 : 1) * SLOT;
          issueW(s + 2, so); issueX(s + 2, so);
        }
        const int t0 = TL ? 4 + 2 * s : 4 + s, t1 = TL ? 5 + 2 * s : 10;
        if (s == 5 && j == 3) { if (lane < 20) glds1(FLA, O_FA + lane * 16, ws); } else g1_touch(t1);
        g1_touch(t0);
        csum((s % 3 == 0 ? 0 : s % 3 == 1 ? 2 : 1) * SLOT, cl);
      }
      wait_vm<16>(); bar();
      {
        const u32x4 v = *(volatile LDS u32x4*)(size_t)(FLA + (lane < 20 ? lane : 0) * 16);
        faok = __all(v[0] == (unsigned)tag && v[2] == (unsigned)tag && v[1] == (unsigned)(tag >> 32) && v[3] == (unsigned)(tag >> 32));
      }
      get_stats(); wd_touch(0); wd_touch(1); wd_touch(2);
      csum(SLOT, cl);
      wait_vm<6>();
      ST(1);
      bar();
      csum(0, cl);
      *(LDS float*)(size_t)(l0 + L_CS + j * 128 + (lane & 31) * 4) = (g & 1) ? cl[1][0] : cl[0][0];
      if (!faok) { poll(ws, O_FA + (lane < 40 ? lane : 0) * 8, tag); get_stats(); wait_vm<0>(); } else wait_vm<3>();
      wait_lgkm0m();
      lds_put(l0 + L_RDY + 4 * j, 1);
      const int pos = 3 * gi + j;
      const bool hasc = j < 3 && pos < 640;
      const unsigned dvb = *(const GAS u16*)(dsk + pos2ch(hasc ? pos : 0));
      ST(2);
      if (j == 3) {
        wd_to_lds(10, 20);
        lds_wait4<4>(l0 + L_RDY, 1);
        lds_wait4<4>(l0 + L_FREE, 1);
        wd_to_lds(0, 10);
      } else if (hasc) {
        const unsigned KB = l0 + L_CONV + j * CONVB, UB = KB + KXB;
        const char* ub = ws + O_XNB + pos * 16;
        #pragma unroll
        for (int i = 0; i < 4; i++) glds1(UB + 512 + i * 1024, lane * 10240, ub + (size_t)i * 64 * 10240);
        *(LDS u32x2*)(size_t)(UB + lane * 8) = u32x2{0, 0};
        poll(ws, O_FK + pos * 8, tag);
        const u32x4 k01 = __builtin_amdgcn_raw_buffer_load_b128(rw, O_KX + pos * KCB + lane * 16, 0, SC1);
        const u32x2 k2 = __builtin_amdgcn_raw_buffer_load_b64(rw, O_KX + pos * KCB + 1024 + lane * 8, 0, SC1);
        kexpand(k01, k2, KB, lane);
        wait_vm<0>(); wait_lgkm0m();
        lds_wait4<4>(l0 + L_FREE, 1);
        ST(4);
        __builtin_amdgcn_s_setprio(1);
        conv(KB, UB, bf_lo(dvb), rw, O_ST + pos2ch(pos) * 4096, lane);
        ST(5);
        publish(ws, O_FS + pos2ch(pos) * 8, tag, lane);
        ST(3);
      }
    } else {
      if (threadIdx.x < 16) *(volatile LDS unsigned*)(size_t)(l0 + L_FULL + 4 * lane) = 0;
      unsigned wa[4], xa[2], wb[4], xb[2];
      #pragma unroll
      for (int u = 0; u < 4; u++) wa[u] = l0 + 32768 + (8 * g + q) * 128 + ((u ^ ((q >> 1) | ((g & 1) << 1))) * 32) + 8 * p;
      #pragma unroll
      for (int k = 0; k < 2; k++) xa[k] = l0 + (w * 64 + (lane & 15)) * 128 + (((4 * k + g) ^ ((lane >> 1) & 7)) * 16);
      f32x4 acc[8][4];
      #pragma unroll
      for (int f = 0; f < 8; f++)
        #pragma unroll
        for (int t = 0; t < 4; t++) { acc[f][t] = f32x4{0, 0, 0, 0}; asm volatile("" : "+v"(acc[f][t])); }
      bf16x8 X[2][4]; s16x4 W[4][2];
      auto rdx = [&](int k, int t) { X[k][t] = *(LDS bf16x8*)((LDS char*)(size_t)xb[k] + t * 2048); };
      auto rdw = [&](int n, int e) { const int k = n >> 3, f = n & 7; W[n & 3][e] = __builtin_amdgcn_ds_read_tr16_b64_v4i16((LDS s16x4*)((LDS char*)(size_t)wb[f >> 1] + (f & 1) * 8192 + k * 4096 + e * 512)); };
      auto mf = [&](int n, int t) { const int k = n >> 3, f = n & 7, tt = (f & 1) ? 3 - t : t;
        mf16(acc[f][tt], __builtin_bit_cast(bf16x8, __builtin_shufflevector(W[n & 3][0], W[n & 3][1], 0, 1, 2, 3, 4, 5, 6, 7)), X[k][tt]); };
      auto base = [&](unsigned& d, unsigned a, unsigned so) { asm volatile("v_add_u32 %0, %1, %2" : "=v"(d) : "i"(so), "v"(a)); };
      auto body = [&](unsigned son) {
        #pragma unroll
        for (int n = 0; n < 13; n++) {
          mf(n, 0); rdw(n + 3, 0); mf(n, 1); rdw(n + 3, 1); mf(n, 2);
          if (n < 4) rdx(1, n);
          if (n == 4) base(xb[0], xa[0], son);
          if (n == 5) base(xb[1], xa[1], son);
          if (n == 7) base(wb[0], wa[0], son);
          if (n == 9) base(wb[1], wa[1], son);
          if (n == 11) base(wb[2], wa[2], son);
          mf(n, 3);
        }
        base(wb[3], wa[3], son);
      };
      #pragma unroll
      for (int u = 0; u < 4; u++) wb[u] = wa[u];
      xb[0] = xa[0]; xb[1] = xa[1];
      GET_TAG(0);
      wait_lgkm0m();
      bar();
      ST(1);
      #pragma unroll
      for (int t = 0; t < 4; t++) rdx(0, t);
      #pragma unroll
      for (int n = 0; n < 3; n++) { rdw(n, 0); rdw(n, 1); }
      body(2 * SLOT);
      #pragma unroll
      for (int s = 1; s < 10; s++) {
        sb(); wait_lgkm0(); bar();
        mf(13, 0); rdw(0, 0); mf(13, 1); rdw(0, 1); mf(13, 2); rdx(0, 0); mf(13, 3); rdx(0, 1);
        mf(14, 0); rdw(1, 0); mf(14, 1); rdw(1, 1); mf(14, 2); rdx(0, 2); mf(14, 3); rdx(0, 3);
        mf(15, 0); rdw(2, 0); mf(15, 1); rdw(2, 1); mf(15, 2); mf(15, 3);
        body(((s + 1) % 3 == 0 ? 0 : (s + 1) % 3 == 1 ? 2 : 1) * SLOT);
      }
      sb(); wait_lgkm0();
      lds_put(l0 + L_FREE + 4 * w, 1);
      #pragma unroll
      for (int n = 13; n < 16; n++) { mf(n, 0); mf(n, 1); mf(n, 2); mf(n, 3); }
      __builtin_amdgcn_s_setprio(2);
      ST(2);
      lds_wait4<1>(l0 + L_RDY, 1);
      float nm[4], k0[4], k1[4], sq[4];
      #pragma unroll
      for (int t = 0; t < 4; t++) {
        unsigned st = *(LDS unsigned*)(size_t)(l0 + L_ST + (w * 64 + t * 16 + (lane & 15)) * 4);
        float den = bf_hi(st), ri = __builtin_amdgcn_rcpf(den);
        nm[t] = -bf_lo(st); k0[t] = -2.3022082f * ri; k1[t] = -2.3022082f * 0.044715f * ri * ri * ri; sq[t] = den * den;
      }
      #pragma unroll
      for (int f = 0; f < 8; f++) asm volatile("s_nop 0" : "+v"(acc[f][0]), "+v"(acc[f][1]), "+v"(acc[f][2]), "+v"(acc[f][3]));
      #pragma unroll
      for (int u = 0; u < 4; u++) {
        const f32x4 cg = *(LDS f32x4*)(size_t)(l0 + L_CS + u * 128 + g * 16), cu = *(LDS f32x4*)(size_t)(l0 + L_CS + u * 128 + 64 + g * 16);
        unsigned o[4][2];
        #pragma unroll
        for (int t = 0; t < 4; t++)
          #pragma unroll
          for (int e = 0; e < 2; e++) {
            f32x2 gg = f32x2{cg[2 * e], cg[2 * e + 1]} * nm[t] + f32x2{acc[2 * u][t][2 * e], acc[2 * u][t][2 * e + 1]};
            f32x2 uu = f32x2{cu[2 * e], cu[2 * e + 1]} * nm[t] + f32x2{acc[2 * u + 1][t][2 * e], acc[2 * u + 1][t][2 * e + 1]};
            f32x2 a = gg * (gg * gg * k1[t] + k0[t]);
            f32x2 ee = {__builtin_amdgcn_exp2f(a[0]), __builtin_amdgcn_exp2f(a[1])};
            ee = ee * sq[t] + sq[t];
            f32x2 hv = gg * uu * f32x2{__builtin_amdgcn_rcpf(ee[0]), __builtin_amdgcn_rcpf(ee[1])};
            o[t][e] = pk2(hv[0], hv[1]);
          }
        #pragma unroll
        for (int k = 0; k < 2; k++) {
          auto r0 = __builtin_amdgcn_permlane16_swap(o[2 * k][0], o[2 * k + 1][0], false, false), r1 = __builtin_amdgcn_permlane16_swap(o[2 * k][1], o[2 * k + 1][1], false, false);
          __builtin_amdgcn_raw_buffer_store_b128(u32x4{r0[0], r1[0], r0[1], r1[1]}, rw, O_HT + (((panel * 8 + w * 2 + k) * 108 + slice * 4 + u) * 64 + lane) * 16, 0, SC1);
        }
      }
      __builtin_amdgcn_s_setprio(0);
      ST(3);
      publish(ws, O_FH + ((panel * 4 + w) * 32 + slice) * 8, tag, lane);
      ST(4);
    }
  } else {
    const int ai = xcd * 5 + r - 27;
    const unsigned S = l0 + w * AUXW;
    auto arrive = [&]() {
      if (lane == 0) *(volatile LDS u64*)(size_t)(l0 + L_FULL + 64 + w * 8) = tag;
      const u64 v = *(volatile LDS u64*)(size_t)(l0 + L_FULL + 64 + (lane & 7) * 8);
      if (__all(v == tag) && lane == 0) __hip_atomic_store((GAS u64*)(ws + O_FA + ai * 8), tag, RLX);
    };
    if (w < 4) {
      int job[2], tb[2];
      #pragma unroll
      for (int i = 0; i < 2; i++) {
        job[i] = (3 + 4 * i - w) * 40 + ai; tb[i] = (w + 4 * i) * 40 + ai;
        if (job[i] < 270) {
          const char* rb = (const char*)wd + (size_t)(job[i] / 5) * 32 * 1280 + (job[i] % 5) * 256;
          #pragma unroll
          for (int k = 0; k < 8; k++) glds(S + i * 8192 + k * 1024, (lane >> 4) * 1280 + (((lane & 15) ^ (2 * ((lane >> 4) | (k & 2) << 1))) * 16), rb + k * 4 * 1280);
        }
      }
      unsigned xv[2][8][5];
      #pragma unroll
      for (int i = 0; i < 2; i++) {
        const GAS unsigned* xp = (const GAS unsigned*)x + min(tb[i], 255) * 2560 + lane;
        if (i == 0 || tb[1] < 256) {
          #pragma unroll
          for (int k = 0; k < 8; k++)
            #pragma unroll
            for (int jj = 0; jj < 5; jj++) xv[i][k][jj] = xp[k * 320 + jj * 64];
        }
      }
      const KPar P = kpar_load(lar, aim, cre, cim, ldt, pos2ch(480 + ai * 4 + w), lane);
      asm volatile("" :: "v"(xv[0][0][0]) : "memory");
      GET_TAG(63);
      ST(9);
      #pragma unroll
      for (int i = 0; i < 2; i++)
        if (job[i] < 270) {
          #pragma unroll
          for (int c = 0; c < 8; c++) {
            unsigned a = S + i * 8192 + (8 * g + q) * 256 + ((c ^ (q | (g & 1) << 2)) * 32) + 8 * p;
            __builtin_amdgcn_raw_buffer_store_b128(__builtin_bit_cast(u32x4, tr2(a, a + 1024)), rw, O_WDT + (((job[i] / 5) * 40 + (job[i] % 5) * 8 + c) * 64 + lane) * 16, 0, SC1);
          }
        }
      ST(11);
      layernorm(xv[0], tb[0], rw, lane);
      if (tb[1] < 256) layernorm(xv[1], tb[1], rw, lane);
      ST(10);
      wait_vm<0>();
      arrive();
      ST(2);
      kgen(P, S, rw, O_KX + (480 + ai * 4 + w) * KCB, lane);
      publish(ws, O_FK + (480 + ai * 4 + w) * 8, tag, lane);
    } else {
      const int p0 = ai * 4 + w - 4;
      KPar P0 = kpar_load(lar, aim, cre, cim, ldt, pos2ch(p0), lane), P1 = kpar_load(lar, aim, cre, cim, ldt, pos2ch(p0 + 160), lane), P2 = kpar_load(lar, aim, cre, cim, ldt, pos2ch(p0 + 320), lane);
      GET_TAG(0);
      arrive();
      ST(2);
      #pragma unroll 1
      for (int c = 0; c < 3; c++) {
        kgen(P0, S, rw, O_KX + (p0 + 160 * c) * KCB, lane);
        P0 = P1; P1 = P2;
        ST(12 + c);
        publish(ws, O_FK + (p0 + 160 * c) * 8, tag, lane);
      }
    }
    ST(3);
    if (w == 7) lds_put(l0 + L_FLAG, 0);
    wait_lgkm0m();
    bar();
    if (w == 7 || w < 4) poll(ws, O_FA + (lane < 40 ? lane : 0) * 8, tag);
    if (w == 7) wd_to_lds(0, 20);
  }

  if (w == 7) {
    poll(ws, O_FH + (rt * 32 + (lane < 27 ? lane : 0)) * 8, tag);
    lds_put(l0 + L_FLAG, 1);
    ST(5);
    poll(ws, O_FS + (cs * 80 + lane) * 8, O_FS + (cs * 80 + 64 + (lane & 15)) * 8, tag);
    #pragma unroll
    for (int i = 0; i < 10; i++) glds1(l0 + L_S + i * 1024, (lane >> 3) * 4096 + (((lane & 7) ^ (2 * ((lane >> 4) & 3))) * 16), ws + O_ST + (size_t)(cs * 80 + i * 8) * 4096 + rt * 128);
    if (b == 255) {
      #pragma unroll 1
      for (int i = 0; i < 4; i++) { int t = i * 64 + lane; poll(ws, t < 216 ? O_FH + ((t / 27) * 128 + t % 27) * 8 : O_FA + (t - 216) * 8, tag); }
      if (lane == 0) __hip_atomic_store((GAS u64*)(ws + O_E), tag, RLX);
    }
    wait_vm<0>();
    ST(6);
    bar();
  } else if (w >= 4) {
    bar();
  } else {
    const unsigned hb = O_HT + ((rt * 2 * 108 + (lane >> 5)) * 64 + ((lane >> 4) & 1) * 32 + (lane & 15)) * 16;
    const unsigned wb = O_WDT + (cs * 5 * 64 + lane) * 16;
    unsigned ks[14];
    #pragma unroll
    for (int i = 0; i < 14; i++) ks[i] = (i == 13 && w >= 2) ? 0x4000u : kidx(w, i);
    auto ldH = [&](int i, int tt) { return __builtin_bit_cast(bf16x8, __builtin_amdgcn_raw_buffer_load_b128(rw, hb + (tt & 1) * 256, ks[i] * 2048 + (tt >> 1) * 110592, SC1)); };
    auto ldW = [&](int i, int c) { return __builtin_bit_cast(bf16x8, __builtin_amdgcn_raw_buffer_load_b128(rw, wb + c * 1024, ks[i] * 40960, SC1)); };
    bf16x8 W[4][5], H[4][4];
    #pragma unroll
    for (int i = 0; i < 4; i++)
      #pragma unroll
      for (int c = 0; c < 5; c++) W[i][c] = ldW(i, c);
    sb();
    lds_wait(l0 + L_FLAG);
    ST(5);
    constexpr int ord[14] = {0, 4, 1, 5, 2, 6, 3, 7, 9, 8, 10, 11, 12, 13};
    constexpr int set[14] = {0, 0, 1, 1, 2, 2, 3, 3, 0, 0, 1, 2, 3, 0};
    #pragma unroll
    for (int i = 0; i < 3; i++)
      #pragma unroll
      for (int t = 0; t < 4; t++) H[i][t] = ldH(ord[i], t);
    f32x4 acc[4][5];
    u32x2 xr[5];
    const unsigned wl = l0 + L_WD + w * WDW + lane * 16;
    auto kstep = [&](int i) {
      const bool inl = ord[i] >= 4 && ord[i] < 9, nxl = i < 13 && ord[i + 1] >= 4 && ord[i + 1] < 9;
      #pragma unroll
      for (int c = 0; c < 5; c++) {
        #pragma unroll
        for (int t = 0; t < 4; t++) {
          const int tt = (c & 1) ? 3 - t : t;
          if (i == 0) mf16z(acc[tt][c], W[set[i]][c], H[i & 3][tt]); else mf16(acc[tt][c], W[set[i]][c], H[i & 3][tt]);
          if (t == 0 && c < 4 && i + 3 < 14) H[(i + 3) & 3][c] = ldH(ord[i + 3], c);
        }
        if (nxl) W[set[i]][c] = ld128(wl + (ord[i + 1] - 4) * 5120 + c * 1024);
        else if (inl) W[set[i]][c] = ldW(ord[i] + 5, c);
        else if (i == 10) xr[c] = *(const GAS u32x2*)(x + (size_t)(rt * 64 + w * 16 + (lane & 15)) * D + cs * 80 + c * 16 + (lane >> 4) * 4);
      }
    };
    #pragma unroll
    for (int i = 0; i < 13; i++) kstep(i);
    if (w < 2) kstep(13);
    mf_pad();
    sb();
    ST(6);
    #pragma unroll
    for (int t = 0; t < 4; t++)
      #pragma unroll
      for (int c = 0; c < 5; c++) *(LDS f32x4*)(size_t)(wl + (t * 5 + c) * 1024) = acc[t][c];
    wait_lgkm0m();
    bar();
    ST(7);
    #pragma unroll
    for (int c = 0; c < 5; c++) {
      f32x4 o = {0, 0, 0, 0};
      #pragma unroll
      for (int v = 0; v < 4; v++) o += *(LDS f32x4*)(size_t)(l0 + L_WD + v * WDW + (w * 5 + c) * 1024 + lane * 16);
      const int row = c * 16 + 4 * g + q;
      u32x2 sv = __builtin_bit_cast(u32x2, trd(l0 + L_S + row * 128 + ((w ^ ((row >> 1) & 3)) * 32) + 8 * p));
      float sf[4] = {bf_lo(sv[0]), bf_hi(sv[0]), bf_lo(sv[1]), bf_hi(sv[1])}, xf[4] = {bf_lo(xr[c][0]), bf_hi(xr[c][0]), bf_lo(xr[c][1]), bf_hi(xr[c][1])}, y[4];
      #pragma unroll
      for (int e = 0; e < 4; e++) y[e] = fmaf(sf[e], o[e] * __builtin_amdgcn_rcpf(1.f + __builtin_amdgcn_exp2f(-1.4426950408889634f * o[e])), xf[e]);
      __builtin_amdgcn_raw_buffer_store_b64(u32x2{pk2(y[0], y[1]), pk2(y[2], y[3])}, __builtin_amdgcn_make_buffer_rsrc(out, 0, L * D * 2, 0x00020000), ((rt * 64 + w * 16 + (lane & 15)) * D + cs * 80 + c * 16 + g * 4) * 2, 0, 2);
    }
    ST(8);
  }
  ST_DUMP();
}

extern "C" void kernel_launch(void* const* d_in, const int* in_sizes, int n_in, void* d_out, int out_size, void* d_ws, size_t ws_size, hipStream_t stream) {
  (void)hipFuncSetAttribute((const void*)fused_kernel, hipFuncAttributeMaxDynamicSharedMemorySize, LDS_BYTES);
  fused_kernel<<<256, 512, LDS_BYTES, stream>>>((const u16*)d_in[0], (const u16*)d_in[1], (const u16*)d_in[2], (const u16*)d_in[3], (const u16*)d_in[4], (const u16*)d_in[5],
      (const u16*)d_in[6], (const u16*)d_in[8], (const u16*)d_in[9], (const u16*)d_in[10], (u16*)d_out, (char*)d_ws);
}
```

```cpp
#include <hip/hip_runtime.h>
#include <stdint.h>

typedef unsigned short u16;
typedef unsigned long long u64;
typedef __attribute__((ext_vector_type(4))) short s16x4;
typedef __attribute__((ext_vector_type(8))) __bf16 bf16x8;
typedef __attribute__((ext_vector_type(2))) __bf16 bf16x2;
typedef __attribute__((ext_vector_type(2))) float f32x2;
typedef __attribute__((ext_vector_type(4))) float f32x4;
typedef __attribute__((ext_vector_type(16))) float f32x16;
typedef __attribute__((ext_vector_type(2))) unsigned u32x2;
typedef __attribute__((ext_vector_type(4))) unsigned u32x4;
#define LDS __attribute__((address_space(3)))
#define GAS __attribute__((address_space(1)))
#define RLX __ATOMIC_RELAXED, __HIP_MEMORY_SCOPE_AGENT
#define SC1 16

constexpr int L = 2048, D = 640, F = 1728;
#define FPS 128
constexpr unsigned O_E = 0, O_FA = 1024, O_FH = 8192  , O_FS = O_FH + 1024 * 128;
constexpr unsigned O_STAT = 524288, O_XNB = 1u << 20, O_WDT = 4u << 20, O_KX = 7u << 20, O_HT = 13u << 20, O_ST = 21u << 20, WS_BYTES = 24u << 20;
constexpr unsigned KXB = 8448, KCB = 1536;
constexpr unsigned TP = 4224;
#define DI __device__ __forceinline__
constexpr unsigned SLOT = 49152;
DI constexpr unsigned wslot(int i) { return i < 11 ? i * 5120 : 98304 + (i - 11) * 5120; }
constexpr unsigned L_CONV = 59136, CONVB = 13056;
constexpr unsigned L_S = 147456;
constexpr unsigned L_KC = L_S;
constexpr unsigned L_CS = 159744;
constexpr unsigned L_ST = 160256;
constexpr unsigned L_FULL = 161280, L_FREE = L_FULL + 16, L_RDY = L_FULL + 32, L_FLAG = L_FULL + 48  , L_DUMP = L_FULL + 256;
constexpr unsigned LDS_BYTES = 163840;
constexpr unsigned AUXW = 20480;
#define SPL 20000
constexpr int SPIN_LIMIT = SPL;

#define ST(i) do {} while (0)
#define ST_INIT() do {} while (0)
#define ST_DUMP() do {} while (0)

DI float bf_lo(unsigned v) { return __uint_as_float(v << 16); }
DI float bf_hi(unsigned v) { return __uint_as_float(v & 0xffff0000u); }
DI unsigned pk2(float a, float b) { return __builtin_bit_cast(unsigned, __builtin_convertvector(f32x2{a, b}, bf16x2)); }
DI float rbf(float a) { return bf_lo(pk2(a, a) & 0xffffu); }

DI void glds(unsigned lds, unsigned voff, const char* sbase) {
  asm volatile("s_mov_b32 m0, %0\n\ts_nop 0\n\tglobal_load_lds_dwordx4 %1, %2" :: "s"(lds), "v"(voff), "s"(sbase) : "memory");
}
DI void glds1(unsigned lds, unsigned voff, const char* sbase) {
  asm volatile("s_mov_b32 m0, %0\n\ts_nop 0\n\tglobal_load_lds_dwordx4 %1, %2 sc1" :: "s"(lds), "v"(voff), "s"(sbase) : "memory");
}
DI void set_m0(unsigned lds) { asm volatile("s_mov_b32 m0, %0\n\ts_nop 0" :: "s"(lds) : "memory"); }
template<int O> DI void gl(unsigned voff, const char* sbase) { asm volatile("global_load_lds_dwordx4 %0, %1 offset:%2" :: "v"(voff), "s"(sbase), "i"(O) : "memory"); }
template<int O> DI void gl1(unsigned voff, const char* sbase) { asm volatile("global_load_lds_dwordx4 %0, %1 offset:%2 sc1" :: "v"(voff), "s"(sbase), "i"(O) : "memory"); }
DI void touch(unsigned lds, const char* p) { asm volatile("s_mov_b32 m0, %0\n\ts_nop 0\n\tglobal_load_lds_dword %1, off" :: "s"(lds), "v"(p) : "memory"); }
DI void touch1(unsigned lds, unsigned voff, const char* sbase) { asm volatile("s_mov_b32 m0, %0\n\ts_nop 0\n\tglobal_load_lds_dword %1, %2 sc1" :: "s"(lds), "v"(voff), "s"(sbase) : "memory"); }
template<int N> DI void wait_vm() { asm volatile("s_waitcnt vmcnt(%0)" :: "i"(N) : "memory"); }
template<int B> DI void wait_vmx(int n) {
  if (n == 0) wait_vm<B>();
  else if (n < 3) { if (n == 1) wait_vm<B + 1>(); else wait_vm<B + 2>(); }
  else if (n < 5) { if (n == 3) wait_vm<B + 3>(); else wait_vm<B + 4>(); }
  else if (n == 5) wait_vm<B + 5>(); else wait_vm<B + 6>();
}
DI void sb() { __builtin_amdgcn_sched_barrier(0); }
DI void bar() { sb(); __builtin_amdgcn_s_barrier(); sb(); }
DI void wait_lgkm0() { __builtin_amdgcn_s_waitcnt(0xC07F); }
DI void wait_lgkm0m() { asm volatile("s_waitcnt lgkmcnt(0)" ::: "memory"); }
DI s16x4 trd(unsigned a) { return __builtin_amdgcn_ds_read_tr16_b64_v4i16((LDS s16x4*)(size_t)a); }
DI bf16x8 tr2(unsigned a, unsigned b) { return __builtin_bit_cast(bf16x8, __builtin_shufflevector(trd(a), trd(b), 0, 1, 2, 3, 4, 5, 6, 7)); }
DI bf16x8 ld128(unsigned a) { return *(LDS bf16x8*)(size_t)a; }
#define MFMA32(a, b, c) __builtin_amdgcn_mfma_f32_32x32x16_bf16(a, b, c, 0, 0, 0)
#define MFMA16(a, b, c) __builtin_amdgcn_mfma_f32_16x16x32_bf16(a, b, c, 0, 0, 0)
#define SGB(m, n) __builtin_amdgcn_sched_group_barrier(m, n, 0)
typedef __amdgpu_buffer_rsrc_t rsrc_t;
DI void mf16(f32x4& c, const bf16x8& a, const bf16x8& b) { asm volatile("v_mfma_f32_16x16x32_bf16 %0, %1, %2, %0" : "+v"(c) : "v"(a), "v"(b)); }
DI void mf16z(f32x4& c, const bf16x8& a, const bf16x8& b) { asm volatile("v_mfma_f32_16x16x32_bf16 %0, %1, %2, 0" : "=&v"(c) : "v"(a), "v"(b)); }
DI void mf_pad() { asm volatile("s_nop 7\n\ts_nop 1" ::: "memory"); }
typedef u32x2 __attribute__((aligned(4))) u32x2a;

DI void poll(const char* ws, unsigned off, unsigned off2, u64 tag) {
  #pragma unroll 1
  for (int it = 0; it < SPIN_LIMIT; it++) {
    const u64 v = __hip_atomic_load((const GAS u64*)(ws + off), RLX), v2 = __hip_atomic_load((const GAS u64*)(ws + off2), RLX);
    if (__all(v == tag && v2 == tag)) break;
    if (__any(v == tag)) __builtin_amdgcn_s_sleep(2); else __builtin_amdgcn_s_sleep(12);
  }
  asm volatile("" ::: "memory");
}
DI void poll(const char* ws, unsigned off, u64 tag) {
  #pragma unroll 1
  for (int it = 0; it < SPIN_LIMIT; it++) {
    const u64 v = __hip_atomic_load((const GAS u64*)(ws + off), RLX);
    if (__all(v == tag)) break;
    if (__any(v == tag)) __builtin_amdgcn_s_sleep(2); else __builtin_amdgcn_s_sleep(12);
  }
  asm volatile("" ::: "memory");
}
DI void publish(char* ws, unsigned off, u64 tag, int lane) {
  asm volatile("s_waitcnt vmcnt(0)" ::: "memory");
  if (lane == 0) __hip_atomic_store((GAS u64*)(ws + off), tag, RLX);
}
DI void lds_wait(unsigned a) {
  #pragma unroll 1
  for (int it = 0; it < 320000; it++) {
    if (__builtin_amdgcn_readfirstlane(*(volatile LDS unsigned*)(size_t)a)) break;
    __builtin_amdgcn_s_sleep(1);
  }
  asm volatile("" ::: "memory");
}
DI unsigned min4(u32x4 v) { return __builtin_amdgcn_readfirstlane(min(min(v[0], v[1]), min(v[2], v[3]))); }
DI unsigned lds_min4(unsigned a) { return min4(*(volatile LDS u32x4*)(size_t)a); }
template<int SL> DI void lds_wait4(unsigned a, unsigned need) {
  #pragma unroll 1
  for (int it = 0; it < 320000 && lds_min4(a) < need; it++) __builtin_amdgcn_s_sleep(SL);
  asm volatile("" ::: "memory");
}
DI void lds_put(unsigned a, unsigned v) { if ((threadIdx.x & 63) == 0) *(volatile LDS unsigned*)(size_t)a = v; }
DI int pos2ch(int pos) { return (pos & ~127) + 2 * (pos & 63) + ((pos >> 6) & 1); }

DI float red8(const float (&a)[8], int lane) {
  float b[4], c[2];
  #pragma unroll
  for (int i = 0; i < 4; i++) { auto r = __builtin_amdgcn_permlane32_swap(__float_as_uint(a[2 * i]), __float_as_uint(a[2 * i + 1]), false, false); b[i] = __uint_as_float(r[0]) + __uint_as_float(r[1]); }
  #pragma unroll
  for (int i = 0; i < 2; i++) { auto r = __builtin_amdgcn_permlane16_swap(__float_as_uint(b[2 * i]), __float_as_uint(b[2 * i + 1]), false, false); c[i] = __uint_as_float(r[0]) + __uint_as_float(r[1]); }
  bool up = lane & 8;
  float keep = up ? c[1] : c[0], send = up ? c[0] : c[1];
  float d = keep + __uint_as_float(__builtin_amdgcn_update_dpp(0, __float_as_uint(send), 0x128, 0xf, 0xf, true));
  d += __uint_as_float(__builtin_amdgcn_update_dpp(0, __float_as_uint(d), 0x141, 0xf, 0xf, true));
  d += __uint_as_float(__builtin_amdgcn_update_dpp(0, __float_as_uint(d), 0xB1, 0xf, 0xf, true));
  d += __uint_as_float(__builtin_amdgcn_update_dpp(0, __float_as_uint(d), 0x4E, 0xf, 0xf, true));
  return d;
}
DI constexpr int red8_lane(int i) { return ((i & 1) << 5) | (((i >> 1) & 1) << 4) | (((i >> 2) & 1) << 3); }
DI float rdl(float v, int l) { return __uint_as_float(__builtin_amdgcn_readlane(__float_as_uint(v), l)); }

DI f32x2 cmul(f32x2 a, f32x2 b) {
  f32x2 r;
  asm("v_pk_mul_f32 %0, %1, %2 op_sel:[1,1] op_sel_hi:[1,0] neg_lo:[0,1]\n\tv_pk_fma_f32 %0, %1, %2, %0 op_sel_hi:[0,1,1]" : "=&v"(r) : "v"(a), "v"(b));
  return r;
}
DI f32x2 zpow(float al, float be, float p) {
  float e = __builtin_amdgcn_exp2f(al * p), f = __builtin_amdgcn_fractf(be * p);
  return f32x2{e * __builtin_amdgcn_cosf(f), e * __builtin_amdgcn_sinf(f)};
}

typedef __attribute__((ext_vector_type(2))) _Float16 h2;
typedef __attribute__((ext_vector_type(8))) _Float16 f16x8;
DI h2 cmulh(h2 a, h2 b) { h2 t = h2{a[1], a[1]} * h2{-b[1], b[0]}; return __builtin_elementwise_fma(h2{a[0], a[0]}, b, t); }
DI h2 toh(f32x2 a) { return __builtin_convertvector(a, h2); }
DI h2 tohc(f32x2 a) { h2 r; asm("v_cvt_pk_f16_f32 %0, %1, -%2" : "=v"(r) : "v"(a[0]), "v"(a[1])); return r; }
template<int C> DI float maxd(float v) { return fmaxf(v, __uint_as_float(__builtin_amdgcn_update_dpp(0, __float_as_uint(v), C, 0xf, 0xf, true))); }

struct KPar { unsigned lar, aim, cre, cim; unsigned ldt; };
DI KPar kpar_load(const u16* lar, const u16* aim, const u16* cre, const u16* cim, const u16* ldt, int ch, int lane) {
  KPar p; int o = ch * 64 + lane;
  p.lar = ((const GAS unsigned*)lar)[o]; p.aim = ((const GAS unsigned*)aim)[o]; p.cre = ((const GAS unsigned*)cre)[o]; p.cim = ((const GAS unsigned*)cim)[o];
  p.ldt = ((const GAS u16*)ldt)[ch];
  return p;
}
DI void kgen(const KPar& P, unsigned S0, unsigned S1, u32x4& o01, u32x2& o2, int lane) {
  const float LOG2E = 1.4426950408889634f;
  const float dt = rbf(__builtin_amdgcn_exp2f(bf_lo(P.ldt) * LOG2E));
  f32x2 C[2] = {f32x2{bf_lo(P.cre), bf_lo(P.cim)}, f32x2{bf_hi(P.cre), bf_hi(P.cim)}};
  float mx = fmaxf(fmaxf(fabsf(C[0][0]), fabsf(C[0][1])), fmaxf(fabsf(C[1][0]), fabsf(C[1][1])));
  mx = maxd<0xB1>(mx); mx = maxd<0x4E>(mx); mx = maxd<0x141>(mx); mx = maxd<0x140>(mx);
  mx = fmaxf(fmaxf(rdl(mx, 0), rdl(mx, 16)), fmaxf(rdl(mx, 32), rdl(mx, 48)));
  const int ex = __builtin_amdgcn_frexp_expf(mx);
  const float cs = __builtin_amdgcn_ldexpf(1.f, -ex), ks = __builtin_amdgcn_ldexpf(2.f * dt, ex);
  h2 T[2][8], Pw[2][4], Q[2][4], U[2][17];
  #pragma unroll
  for (int m = 0; m < 2; m++) {
    float la = m ? bf_hi(P.lar) : bf_lo(P.lar), ai = m ? bf_hi(P.aim) : bf_lo(P.aim);
    float al = -rbf(__builtin_amdgcn_exp2f(la * LOG2E)) * dt * LOG2E, be = ai * dt * 0.15915494309189535f;
    f32x2 z1 = zpow(al, be, 1.f), u1 = zpow(al, be, 128.f);
    f32x2 z2 = cmul(z1, z1), z3 = cmul(z2, z1), z4 = cmul(z2, z2), z8 = cmul(z4, z4), z16 = cmul(z8, z8), z24 = cmul(z16, z8), z32 = cmul(z16, z16);
    f32x2 q0 = C[m] * cs, q1 = cmul(q0, z32), q2 = cmul(q1, z32), q3 = cmul(q2, z32);
    f32x2 u2 = cmul(u1, u1), u3 = cmul(u2, u1), u4 = cmul(u2, u2), u8 = cmul(u4, u4), u12 = cmul(u8, u4), u16 = cmul(u8, u8);
    T[m][0] = h2{1, 0}; T[m][1] = toh(z1); T[m][2] = toh(z2); T[m][3] = toh(z3); T[m][4] = toh(z4);
    Pw[m][1] = tohc(z8); Pw[m][2] = tohc(z16); Pw[m][3] = tohc(z24);
    Q[m][0] = tohc(q0); Q[m][1] = tohc(q1); Q[m][2] = tohc(q2); Q[m][3] = tohc(q3);
    U[m][1] = toh(u1); U[m][2] = toh(u2); U[m][3] = toh(u3); U[m][4] = toh(u4); U[m][8] = toh(u8); U[m][12] = toh(u12); U[m][16] = toh(u16);
  }
  const unsigned wa[2] = {S0 + lane * 8, S1 + lane * 8}, ra[2] = {S0 + (lane & 15) * 528 + (lane >> 4) * 16, S1 + (lane & 15) * 528 + (lane >> 4) * 16};
  auto put2 = [&](int buf, int row, h2 a0, h2 a1, h2 b0, h2 b1) {
    asm volatile("ds_write2_b64 %0, %1, %2 offset0:%3 offset1:%4" :: "v"(wa[buf] + (row & ~3) * 528), "v"(u32x2{__builtin_bit_cast(unsigned, a0), __builtin_bit_cast(unsigned, a1)}),
                 "v"(u32x2{__builtin_bit_cast(unsigned, b0), __builtin_bit_cast(unsigned, b1)}), "i"((row & 3) * 66), "i"((row & 3) * 66 + 66) : "memory");
  };
  auto frag = [&](int buf, int k) { return *(LDS f16x8*)((LDS char*)(size_t)ra[buf] + k * 64); };
  #define CM(X, i, Y, j) cmulh(X[0][i], Y[0][j]), cmulh(X[1][i], Y[1][j])
  #define ID(X, i) X[0][i], X[1][i]
  #pragma unroll
  for (int a = 0; a < 4; a++) { put2(0, 4 * a, ID(Q, a), CM(Q, a, Pw, 1)); put2(0, 4 * a + 2, CM(Q, a, Pw, 2), CM(Q, a, Pw, 3)); }
  #pragma unroll
  for (int e = 5; e < 8; e++) { T[0][e] = cmulh(T[0][4], T[0][e - 4]); T[1][e] = cmulh(T[1][4], T[1][e - 4]); }
  #pragma unroll
  for (int e = 0; e < 8; e += 2) put2(1, e, ID(T, e), ID(T, e + 1));
  f16x8 fine[8], cf[8];
  #pragma unroll
  for (int k = 0; k < 8; k++) fine[k] = frag(0, k);
  put2(1, 8, ID(U, 1), CM(T, 1, U, 1));
  #pragma unroll
  for (int e = 2; e < 8; e += 2) put2(1, 8 + e, CM(T, e, U, 1), CM(T, e + 1, U, 1));
  #pragma unroll
  for (int a = 2; a < 4; a++) { put2(0, 4 * (a - 2), ID(U, a), CM(T, 2, U, a)); put2(0, 4 * (a - 2) + 2, CM(T, 4, U, a), CM(T, 6, U, a)); }
  #pragma unroll
  for (int k = 0; k < 8; k++) cf[k] = frag(1, k);
  f32x4 acc[3][2];
  auto mm = [&](int t, int k) { acc[t][k & 1] = __builtin_amdgcn_mfma_f32_16x16x32_f16(cf[k], fine[k], k < 2 ? f32x4{0, 0, 0, 0} : acc[t][k & 1], 0, 0, 0); };
  #pragma unroll
  for (int a = 4; a < 8; a++) {
    if (a > 4) { U[0][a] = cmulh(U[0][4], U[0][a - 4]); U[1][a] = cmulh(U[1][4], U[1][a - 4]); }
    put2(0, 8 + 2 * (a - 4), ID(U, a), CM(T, 4, U, a));
    mm(0, 2 * (a - 4)); mm(0, 2 * (a - 4) + 1);
  }
  #pragma unroll
  for (int k = 0; k < 8; k++) cf[k] = frag(0, k);
  #pragma unroll
  for (int a = 8; a < 16; a += 4) {
    put2(1, a - 8, ID(U, a), CM(U, a, U, 1)); mm(1, a - 8); mm(1, a - 7);
    put2(1, a - 6, CM(U, a, U, 2), CM(U, a, U, 3)); mm(1, a - 6); mm(1, a - 5);
  }
  put2(1, 8, ID(U, 16), ID(U, 16));
  #pragma unroll
  for (int k = 0; k < 8; k++) cf[k] = frag(1, k);
  const f32x4 k0 = (acc[0][0] + acc[0][1]) * ks, k1 = (acc[1][0] + acc[1][1]) * ks;
  o01 = u32x4{pk2(k0[0], k0[1]), pk2(k0[2], k0[3]), pk2(k1[0], k1[1]), pk2(k1[2], k1[3])};
  #pragma unroll
  for (int k = 0; k < 8; k++) mm(2, k);
  const f32x4 k2 = (acc[2][0] + acc[2][1]) * ks;
  o2 = u32x2{pk2(k2[0], k2[1]), pk2(k2[2], k2[3])};
  #undef CM
  #undef ID
}
DI unsigned prm(unsigned hi, unsigned lo, unsigned sel) { return __builtin_amdgcn_perm(hi, lo, sel); }
DI void kexpand(u32x4 k01, u32x2 k2, unsigned S, int lane) {
  const int b = lane & 15, g = lane >> 4;
  const unsigned TR = S + 4608 + lane * 16;
  const unsigned LL = 0x01000100u, HH = 0x03020302u;
  unsigned a3 = g < 2 ? S + 8 + 2 * (2044 - (1024 + 512 * g + 384 + 8 * b)) : TR;
  unsigned a4 = (g < 2 && !(b & 1)) ? a3 - 2 : TR;
  unsigned v3[4];
  #pragma unroll
  for (int j = 0; j < 4; j++) { v3[j] = prm(0, k2[j >> 1], (j & 1) ? HH : LL); *(LDS u32x4*)((LDS char*)(size_t)a3 + 256 * (3 - j)) = u32x4{v3[j], v3[j], v3[j], v3[j]}; }
  *(LDS u32x4*)(size_t)(lane == 32 ? S : TR) = u32x4{v3[0], v3[0], v3[0], v3[0]};
  #pragma unroll
  for (int j = 0; j < 4; j++) *(volatile LDS u16*)((LDS char*)(size_t)a4 + 256 * (3 - j)) = (u16)v3[j];
  unsigned a2 = g >= 2 ? S + 8 + 2 * (2042 - (512 + 256 * (g - 2) + 128 + 8 * b)) : TR;
  const unsigned l0_ = prm(0, k01[2], LL), h0_ = prm(0, k01[2], HH), l1_ = prm(0, k01[3], LL), h1_ = prm(0, k01[3], HH);
  #pragma unroll
  for (int h = 0; h < 2; h++) {
    volatile LDS unsigned* q = (volatile LDS unsigned*)((LDS char*)(size_t)a2 + 256 * (1 - h));
    q[0] = h ? h1_ : h0_; q[1] = prm(0, k01[2 + h], 0x01000302u); q[2] = h ? l1_ : l0_; q[3] = h ? l1_ : l0_;
  }
  LDS char* a1 = (LDS char*)(size_t)(g < 2 ? S + 8 + 2 * (2041 - (256 + 128 * g + 8 * b)) : TR + 2);
  *(volatile LDS u16*)a1 = (u16)h1_;
  *(volatile LDS unsigned*)(a1 + 2) = l1_;
  *(volatile LDS unsigned*)(a1 + 6) = prm(k01[2], k01[3], 0x07060100u);
  *(volatile LDS unsigned*)(a1 + 10) = l0_;
  *(volatile LDS u16*)(a1 + 14) = (u16)l0_;
  unsigned a0 = S + 8 + 2 * (2044 - (128 * (g >> 1) + 4 * (g & 1) + 8 * b));
  *(volatile LDS u32x2*)(size_t)a0 = u32x2{prm(0, k01[1], 0x01000302u), prm(0, k01[0], 0x01000302u)};
  *(volatile LDS u32x2*)(size_t)(lane < 6 ? S + 8 + 4096 + 8 * lane : TR) = u32x2{0, 0};
  asm volatile("" ::: "memory");
  u32x2 x[5], y[5]; unsigned z[5];
  #pragma unroll
  for (int i = 0; i < 5; i++) {
    LDS char* a = (LDS char*)(size_t)(S + 8 + 16 * (i < 4 ? lane : (lane & 1))) + 1024 * i;
    x[i] = *(LDS u32x2*)a; y[i] = *(LDS u32x2*)(a + 8); z[i] = *(LDS unsigned*)(a + 16);
  }
  asm volatile("" ::: "memory");
  #pragma unroll
  for (int i = 0; i < 5; i++) {
    LDS char* a = (LDS char*)(size_t)(S + 4168 + 16 * (i < 4 ? lane : (lane & 1))) + 1024 * i;
    if (i < 4 || lane < 2) {
      *(LDS u32x2*)a = u32x2{__builtin_amdgcn_alignbit(x[i][1], x[i][0], 16), __builtin_amdgcn_alignbit(y[i][0], x[i][1], 16)};
      *(LDS u32x2*)(a + 8) = u32x2{__builtin_amdgcn_alignbit(y[i][1], y[i][0], 16), __builtin_amdgcn_alignbit(z[i], y[i][1], 16)};
    }
  }
  asm volatile("" ::: "memory");
}

#define CONVU 1
constexpr int CONV_U = CONVU;
DI void conv(unsigned KB, unsigned UB, float dsk, rsrc_t rw, unsigned goff, int lane) {
  const int r = lane & 15, kg = lane >> 4;
  const int n0 = 2047 - (16 * 112 + 16 * (kg >> 1) + r - 8 * (kg & 1));
  unsigned ka = KB + 8 + ((n0 & 1) ? 4160 + 2 * (n0 - 1) : 2 * n0);
  unsigned ua = UB + 2 * (256 + 16 * r - 16 * (kg >> 1) + 8 * (kg & 1));
  f32x4 acc[8];
  #pragma unroll
  for (int t = 0; t < 8; t++) { acc[t] = f32x4{0, 0, 0, 0}; asm volatile("" : "+v"(acc[t])); }
  u32x2 A[2][8][2]; bf16x8 Fr[2][8];
  unsigned kb[4];
  #pragma unroll
  for (int i = 0; i < 4; i++) kb[i] = ka + (3 - i) * 1024 - 64;
  ua -= 64;
  auto ld1 = [&](int s, int n, int e) {
    const int q = n / 3;
    if (n % 3 < 2) A[s][q][n % 3] = *(LDS u32x2a*)((LDS char*)(size_t)kb[q >> 1] + ((q & 1) ? 0 : 512) + e + 8 * (n % 3));
    else Fr[s][q] = *(LDS bf16x8*)((LDS char*)(size_t)ua + q * 512 + e);
  };
  auto dec = [&](unsigned& v) { asm volatile("v_subrev_u32 %0, %1, %0" : "+v"(v) : "s"(128u)); };
  auto step = [&](int s, bool nxt) {
    int n = 0;
    #pragma unroll
    for (int q = 0; q < 8; q++)
      #pragma unroll
      for (int m = 7 - q; m >= 0; m--) {
        mf16(acc[q + m], __builtin_bit_cast(bf16x8, __builtin_shufflevector(A[s][q][0], A[s][q][1], 0, 1, 2, 3)), Fr[s][m]);
        if (n < 24 && nxt) ld1(s ^ 1, n, s ? 64 : 0);
        if (s == 0 && n >= 31) dec(n == 35 ? ua : kb[n - 31]);
        n++;
      }
  };
  #pragma unroll
  for (int i = 0; i < 4; i++) asm volatile("" : "+v"(kb[i]));
  asm volatile("" : "+v"(ua));
  #pragma unroll
  for (int n = 0; n < 24; n++) ld1(0, n, 64);
  #pragma unroll CONV_U
  for (int it = 0; it < 3; it++) { step(0, true); step(1, true); }
  step(0, true);
  step(1, false);
  mf_pad();
  const unsigned ue = UB + 2 * (256 + 16 * r + 4 * kg);
  #pragma unroll
  for (int t = 0; t < 8; t++) {
    u32x2 u = *(LDS u32x2*)(size_t)(ue + 512 * t);
    u32x2 o = {pk2(fmaf(bf_lo(u[0]), dsk, acc[t][0]), fmaf(bf_hi(u[0]), dsk, acc[t][1])), pk2(fmaf(bf_lo(u[1]), dsk, acc[t][2]), fmaf(bf_hi(u[1]), dsk, acc[t][3]))};
    __builtin_amdgcn_raw_buffer_store_b64(o, rw, goff + 512 * t + 32 * r + 8 * kg, 0, SC1);
  }
}

DI void layernorm(const unsigned (&xv)[8][5], int tb, unsigned img, rsrc_t rw, int lane) {
      float s8[8];
      const bf16x2 one2 = __builtin_bit_cast(bf16x2, 0x3f803f80u);
      #pragma unroll
      for (int i = 0; i < 8; i++) { float s = 0;
        #pragma unroll
        for (int jj = 0; jj < 5; jj++) s = __builtin_amdgcn_fdot2_f32_bf16(__builtin_bit_cast(bf16x2, xv[i][jj]), one2, s, false);
        s8[i] = s; }
      const float mean = red8(s8, lane) * (1.f / D);
      f32x2 dv[8][5];
      #pragma unroll
      for (int i = 0; i < 8; i++) {
        const float mi = rdl(mean, red8_lane(i));
        f32x2 qq = {0, 0};
        #pragma unroll
        for (int jj = 0; jj < 5; jj++) { dv[i][jj] = f32x2{bf_lo(xv[i][jj]), bf_hi(xv[i][jj])} - mi; qq += dv[i][jj] * dv[i][jj]; }
        s8[i] = qq[0] + qq[1];
      }
      const float var = rbf(red8(s8, lane) * (1.f / D));
      const float den = rbf(__builtin_sqrtf(rbf(var + 1e-5f))), mb = rbf(mean);
      const float ri = 1.f / den, off = (mean - mb) * ri;
      if ((lane & 7) == 0) __builtin_amdgcn_raw_buffer_store_b32(pk2(mb, den), rw, O_STAT + (tb * 8 + ((lane >> 5) | (((lane >> 4) & 1) << 1) | (((lane >> 3) & 1) << 2))) * 4, 0, SC1);
      #pragma unroll
      for (int i = 0; i < 8; i++) {
        const float rr = rdl(ri, red8_lane(i)), oo = rdl(off, red8_lane(i));
        #pragma unroll
        for (int jj = 0; jj < 5; jj++) dv[i][jj] = dv[i][jj] * rr + oo;
      }
      #pragma unroll
      for (int jj = 0; jj < 5; jj++)
        #pragma unroll
        for (int e = 0; e < 2; e++)
          *(LDS u32x4*)((LDS char*)(size_t)(img + lane * 128 + (((tb ^ lane) & 7) << 4)) + (jj * 128 + e * 64) * 128) =
              u32x4{pk2(dv[0][jj][e], dv[1][jj][e]), pk2(dv[2][jj][e], dv[3][jj][e]), pk2(dv[4][jj][e], dv[5][jj][e]), pk2(dv[6][jj][e], dv[7][jj][e])};
}

__global__ __launch_bounds__(512, 2) void fused_kernel(const u16* x, const u16* lar, const u16* aim, const u16* cre,
    const u16* cim, const u16* ldt, const u16* dsk, const u16* wg, const u16* wu, const u16* wd,
    u16* out, char* ws) {
  extern __shared__ __attribute__((aligned(16))) char smem[];
  const int b = blockIdx.x, xcd = b & 7, r = b >> 3;
  const int w = __builtin_amdgcn_readfirstlane(threadIdx.x >> 6), lane = threadIdx.x & 63;
  const unsigned l0 = (unsigned)(size_t)(LDS char*)smem;
  asm volatile("" : "+s"(x), "+s"(wg), "+s"(wu), "+s"(wd), "+s"(ws), "+s"(out), "+s"(dsk), "+s"(lar), "+s"(aim), "+s"(cre), "+s"(cim), "+s"(ldt));
  const rsrc_t rw = __builtin_amdgcn_make_buffer_rsrc(ws, 0, WS_BYTES, 0x00020000);
  ST_INIT(); ST(0);
  const bool gemm = r < 27;
  const int gi = xcd * 27 + r;
  const int id4 = (xcd & 3) * 27 + r, slice = id4 >> 2, panel = (xcd >> 2) * 4 + (id4 & 3);
#define AUXT 1
#define WDE 1
#define OUTAUX 2
#define AUXS 40
#define XLD 8
#define PFM 1
#define PFD 15
#define G2MAP 0
  const int rt = G2MAP ? (xcd >> 1) * 8 + (r >> 2) : xcd * 4 + (r >> 3), cs = G2MAP ? (xcd & 1) * 4 + (r & 3) : r & 7;
  u32x2 e0;
  asm volatile("global_load_dwordx2 %0, %1, %2 sc1" : "=&v"(e0) : "v"(0u), "s"(ws + O_E) : "memory");
  #define GET_TAG(N) do { unsigned lo_, hi_; asm volatile("s_waitcnt vmcnt(" #N ")\n\tv_readfirstlane_b32 %0, %2\n\tv_readfirstlane_b32 %1, %3" : "=&s"(lo_), "=&s"(hi_) : "v"(e0[0]), "v"(e0[1]) : "memory"); \
    tag = (((u64)hi_ << 32) | lo_) * 6364136223846793005ull + 1442695040888963407ull; } while (0)
  u64 tag;

  const int rot = G2MAP ? ((rt & 7) * 3 + (cs & 3) * 6) >> 1 : (cs * 3 + (rt & 3) * 6) >> 1;
  auto kidx = [&](int wv, int i) { int v = i + rot; v -= v >= 24 ? 24 : v >= 12 ? 12 : 0; return 12 * wv + v; };
  auto kidx7 = [&](int i) { return 48 + (i + rot) % 6; };
  auto wd_to_lds = [&](int i0, int i1) {
    #pragma unroll 1
    for (int i = i0; i < i1; i++) {
      const int cw = (i * 13) >> 6;
      const char* src = ws + O_WDT + 2048 + (kidx(cw, 4 + i - cw * 5) * 40 + cs * 5) * 1024;
      set_m0(l0 + 2048 + i * 5120 + (i < 11 ? 0 : 98304 - 56320));
      gl1<-2048>(lane * 16, src); gl1<-1024>(lane * 16, src); gl1<0>(lane * 16, src); gl1<1024>(lane * 16, src); gl1<2048>(lane * 16, src);
    }
  };
  const unsigned wvo = O_WDT + (cs * 5 * 64 + lane) * 16;
  auto ldWs = [&](int s, int c) { return __builtin_bit_cast(bf16x8, __builtin_amdgcn_raw_buffer_load_b128(rw, wvo + c * 1024, s * 40960, SC1)); };
  const int g = lane >> 4, q = (lane & 15) >> 2, p = lane & 3;
  const unsigned hb = O_HT + ((rt * 2 * 108 + (lane >> 5)) * 64 + ((lane >> 4) & 1) * 32 + (lane & 15)) * 16;
  auto ldHs = [&](int s, int tt) { return __builtin_bit_cast(bf16x8, __builtin_amdgcn_raw_buffer_load_b128(rw, hb + (tt & 1) * 256, s * 2048 + (tt >> 1) * 110592, SC1)); };
  const int tt_ = w & 3, c0 = w < 4 ? 0 : 3;
  u32x2 xr[3];
  auto load_x = [&]() {
    #pragma unroll
    for (int c = 0; c < 3; c++) xr[c] = *(const GAS u32x2*)(x + (size_t)(rt * 64 + tt_ * 16 + (lane & 15)) * D + cs * 80 + min(c0 + c, 4) * 16 + g * 4);
  };
  auto put_acc = [&](unsigned a, const f32x4 (&acc)[4][5]) {
    #pragma unroll
    for (int t = 0; t < 4; t++)
      #pragma unroll
      for (int c = 0; c < 5; c++) *(LDS f32x4*)((LDS char*)(size_t)(a + lane * 16) + (t * 5 + c) * 1024) = acc[t][c];
  };
  auto rt_load = [&](int id, unsigned R) {
    const char* rb = (const char*)wd + (id / 10) * 40960 + (id % 10) * 128;
    const unsigned o = (lane >> 3) * 1280 + (((lane & 7) ^ (2 * ((lane >> 4) & 1))) << 4);
    set_m0(R);
    gl<0>(o, rb); gl<1024>((o ^ 64) + 9216, rb); gl<2048>(o + 18432, rb); gl<3072>((o ^ 64) + 27648, rb);
  };
  auto rt_store = [&](int id, unsigned R) {
    #pragma unroll
    for (int c = 0; c < 4; c++) {
      const unsigned a = R + (8 * g + q) * 128 + ((c ^ ((q >> 1) | ((g & 1) << 1))) * 32) + 8 * p;
      __builtin_amdgcn_raw_buffer_store_b128(__builtin_bit_cast(u32x4, tr2(a, a + 512)), rw, O_WDT + (((id / 10) * 40 + (id % 10) * 4 + c) * 64 + lane) * 16, 0, SC1);
    }
  };
  auto g2_wave7 = [&](bool rest) {
    bf16x8 W7[6][5];
    #pragma unroll
    for (int i = 0; i < 6; i++) {
      if (i == 3 && rest) { ST(2); bar(); }
      #pragma unroll
      for (int c = 0; c < 5; c++) W7[i][c] = ldWs(kidx7(i), c);
    }
    lds_put(l0 + L_RDY + 12, 1);
    if (rest) { wd_to_lds(0, 11); bar(); }
    poll(ws, O_FH + (rt * 32 + (lane < 27 ? lane : 0)) * FPS, tag);
    lds_put(l0 + L_FLAG, 1);
    __builtin_amdgcn_s_setprio(1);
    ST(5);
    bf16x8 H[3][4];
    #pragma unroll
    for (int i = 0; i < 2; i++)
      #pragma unroll
      for (int t = 0; t < 4; t++) H[i][t] = ldHs(kidx7(i), t);
    f32x4 acc[4][5];
    #pragma unroll
    for (int i = 0; i < 6; i++)
      #pragma unroll
      for (int c = 0; c < 5; c++)
        #pragma unroll
        for (int t = 0; t < 4; t++) {
          const int tt = (c & 1) ? 3 - t : t;
          if (i == 0) mf16z(acc[tt][c], W7[i][c], H[i % 3][tt]); else mf16(acc[tt][c], W7[i][c], H[i % 3][tt]);
          if (t == 3 && c < 4 && i + 2 < 6) H[(i + 2) % 3][c] = ldHs(kidx7(i + 2), c);
        }
    load_x();
    mf_pad();
    lds_wait4<2>(l0 + L_RDY, 1);
    put_acc(l0 + L_CONV, acc);
    ST(6);
  };

  if (gemm) {
    if (w >= 4) {
      const int j = w - 4, gu = j & 1;
      unsigned xo[8], wo[4];
      #pragma unroll
      for (int i = 0; i < 8; i++) xo[i] = (lane >> 3) * 1280 + (((lane & 7) ^ ((4 * i + (lane >> 4)) & 7)) * 16) + 9216 * i + 4096;
      #pragma unroll
      for (int i = 0; i < 4; i++) wo[i] = (lane >> 3) * 3456 + (((lane & 7) ^ (2 * (((lane >> 4) & 1) | ((i & 1) << 1)))) * 16) + 26624 * i;
      const char* xb = (const char*)x + (size_t)(panel * 256 + j * 64) * 1280;
      const char* wb = (const char*)(gu ? wu : wg) + (size_t)(32 * (j >> 1)) * 3456 + slice * 128;
      const unsigned lw = l0 + 32768 + gu * 8192 + (j >> 1) * 4096, lx = l0 + j * 8192 + 4096;
      auto issue = [&](unsigned so) {
        set_m0(lw + so);
        gl<0>(wo[0], wb); gl<1024>(wo[1], wb); gl<2048>(wo[2], wb); gl<3072>(wo[3], wb);
        set_m0(lx + so);
        gl<-4096>(xo[0], xb); gl<-3072>(xo[1], xb); gl<-2048>(xo[2], xb); gl<-1024>(xo[3], xb); gl<0>(xo[4], xb); gl<1024>(xo[5], xb); gl<2048>(xo[6], xb); gl<3072>(xo[7], xb);
        xb += 128; wb += 64 * 3456;
      };
      const unsigned FLA = l0 + L_S + 4608;
      auto flags_ok = [&](unsigned a, int n) {
        const u32x4 v = *(volatile LDS u32x4*)(size_t)(a + (lane < n ? lane : 0) * 16);
        return __all(v[0] == (unsigned)tag && v[2] == (unsigned)tag && v[1] == (unsigned)(tag >> 32) && v[3] == (unsigned)(tag >> 32));
      };
      issue(0); issue(2 * SLOT);
      GET_TAG(12);
      ST(8);
      bar();
      issue(SLOT);
      unsigned so = 0;
      #pragma unroll 1
      for (int s = 1; s < 8; s++) {
        wait_vm<12>();
        bar();
        if (s == 7 && j == 3 && lane < 20) glds1(FLA, O_FA + lane * 16, ws);
        issue(so);
        so = so == 0 ? 2 * SLOT : so - SLOT;
      }
      wait_vm<12>(); bar();
      const bool faok = flags_ok(FLA, 20);
      const int pos = 3 * gi + j;
      const bool hasc = j < 3 && pos < 640;
      const unsigned KB = l0 + L_CONV + j * CONVB, UB = KB + KXB;
      auto get_stats = [&]() { touch1(l0 + L_ST + j * 256, O_STAT + (panel * 256 + j * 64 + lane) * 4, ws); };
      get_stats();
      wait_vm<1>();
      if (faok && j < 3) wd_to_lds(11 + 3 * j, 13 + 3 * j);
      ST(1);
      bar();
      if (!faok) { poll(ws, O_FA + (lane < 40 ? lane : 0) * 8, tag); get_stats(); wait_vm<0>(); } else if (j < 3) wait_vm<10>(); else wait_vm<0>();
      if (j == 3) {
        g2_wave7(true);
      } else {
        if (hasc) {
          const char* ub = ws + O_XNB + pos * TP;
          set_m0(UB + 512);
          gl1<0>(lane * 16, ub); gl1<1024>(lane * 16, ub); gl1<2048>(lane * 16, ub); gl1<3072>(lane * 16, ub);
          *(LDS u32x2*)(size_t)(UB + lane * 8) = u32x2{0, 0};
        }
        ST(2);
        bar();
        wd_to_lds(faok ? 13 + 3 * j : 11 + 3 * j, 14 + 3 * j);
        const unsigned dvb = *(const GAS u16*)(dsk + pos2ch(hasc ? pos : 0));
        load_x();
        if (hasc) kexpand(*(LDS u32x4*)(size_t)(l0 + L_KC + j * 1536 + lane * 16), *(LDS u32x2*)(size_t)(l0 + L_KC + j * 1536 + 1024 + lane * 8), KB, lane);
        wait_vm<0>(); wait_lgkm0m();
        bar();
        ST(4);
        if (hasc) {
          __builtin_amdgcn_s_setprio(1);
          conv(KB, UB, bf_lo(dvb), rw, O_ST + pos2ch(pos) * TP, lane);
        }
        ST(5);
        lds_put(l0 + L_RDY + 4 * j, 1);
        wait_vm<0>();
        unsigned n = 0;
        if (lane == 0) n = __hip_atomic_fetch_add((LDS unsigned*)(size_t)(l0 + L_FLAG + 4), 1u, __ATOMIC_RELAXED, __HIP_MEMORY_SCOPE_WORKGROUP);
        if (lane == 0 && n == 2) __hip_atomic_store((GAS u64*)(ws + O_FS + gi * FPS), tag, RLX);
        ST(3);
      }
    } else {
      if (threadIdx.x < 16) *(volatile LDS unsigned*)(size_t)(l0 + L_FULL + 4 * lane) = 0;
      const bool hask = w < 3 && 3 * gi + w < 640;
      const KPar P = kpar_load(lar, aim, cre, cim, ldt, pos2ch(hask ? 3 * gi + w : 0), lane);
      unsigned wa[4], xa[2], wb[4], xb[2];
      #pragma unroll
      for (int u = 0; u < 4; u++) wa[u] = l0 + 32768 + (8 * g + q) * 128 + ((((u + w) & 3) ^ ((q >> 1) | ((g & 1) << 1))) * 32) + 8 * p;
      #pragma unroll
      for (int k = 0; k < 2; k++) xa[k] = l0 + (w * 64 + (lane & 15)) * 128 + (((4 * k + g) ^ ((lane >> 1) & 7)) * 16);
      f32x4 acc[8][4];
      f32x4 cacc[2];
      bf16x8 ones = __builtin_bit_cast(bf16x8, u32x4{0x3f803f80u, 0x3f803f80u, 0x3f803f80u, 0x3f803f80u});
      asm volatile("" : "+v"(ones));
      bf16x8 X[2][4]; s16x4 W[4][2];
      auto rdx = [&](int k, int t) { X[k][t] = *(LDS bf16x8*)((LDS char*)(size_t)xb[k] + t * 2048); };
      auto rdw = [&](int n, int e) { const int k = n >> 3, f = n & 7; W[n & 3][e] = __builtin_amdgcn_ds_read_tr16_b64_v4i16((LDS s16x4*)((LDS char*)(size_t)wb[f >> 1] + (f & 1) * 8192 + k * 4096 + e * 512)); };
      auto wf = [&](int n) { return __builtin_bit_cast(bf16x8, __builtin_shufflevector(W[n & 3][0], W[n & 3][1], 0, 1, 2, 3, 4, 5, 6, 7)); };
      auto mf = [&](int n, int t, bool z = false) { const int k = n >> 3, f = n & 7, tt = (f & 1) ? 3 - t : t;
        if (z) mf16z(acc[f][tt], wf(n), X[k][tt]); else mf16(acc[f][tt], wf(n), X[k][tt]); };
      auto base = [&](unsigned& d, unsigned a, unsigned so) { asm volatile("v_add_u32 %0, %1, %2" : "=v"(d) : "i"(so), "v"(a)); };
      auto body = [&](unsigned son, bool first) {
        #pragma unroll
        for (int n = 0; n < 13; n++) {
          const bool z = first && n < 8;
          mf(n, 0, z); rdw(n + 3, 0); mf(n, 1, z); rdw(n + 3, 1); mf(n, 2, z);
          if (n < 4) rdx(1, n);
          if (n == 4) base(xb[0], xa[0], son);
          if (n == 5) base(xb[1], xa[1], son);
          if (n == 7) base(wb[0], wa[0], son);
          if (n == 9) base(wb[1], wa[1], son);
          if (n == 11) base(wb[2], wa[2], son);
          mf(n, 3, z);
          if ((n & 6) == 0) { if (z) mf16z(cacc[n & 1], wf(n), ones); else mf16(cacc[n & 1], wf(n), ones); }
        }
        base(wb[3], wa[3], son);
      };
      #pragma unroll
      for (int u = 0; u < 4; u++) wb[u] = wa[u];
      xb[0] = xa[0]; xb[1] = xa[1];
      asm volatile("" : "+v"(wb[0]), "+v"(wb[1]), "+v"(wb[2]), "+v"(wb[3]), "+v"(xb[0]), "+v"(xb[1]));
      GET_TAG(0);
      ST(8);
      u32x4 o01 = {0, 0, 0, 0}; u32x2 o2 = {0, 0};
      if (hask) {
        kgen(P, l0 + SLOT + w * 8448, w < 2 ? l0 + SLOT + (3 + w) * 8448 : l0 + L_S, o01, o2, lane);
      } else if (w == 3) {
        if (PFD) __builtin_amdgcn_s_sleep(PFD);
        const int s0 = ((xcd & 3) * 27) >> 2, ns = (((xcd & 3) * 27 + 26) >> 2) - s0 + 1;
        #pragma unroll
        for (int i = 0; i < 3; i++) {
          const int n = (i * 27 + r) * 51 + min(lane, 50), k = min(128 + n / ns, 639), c = n % ns;
          if (lane < 51) touch(l0 + L_DUMP, (const char*)((xcd >> 2) ? wu : wg) + (size_t)k * 3456 + (s0 + c) * 128);
        }
      }
      ST(9);
      wait_lgkm0m();
      bar();
      ST(1);
      #pragma unroll
      for (int t = 0; t < 4; t++) rdx(0, t);
      #pragma unroll
      for (int n = 0; n < 3; n++) { rdw(n, 0); rdw(n, 1); }
      body(2 * SLOT, true);
      #pragma unroll
      for (int s = 1; s < 10; s++) {
        sb(); wait_lgkm0(); bar();
        mf(13, 0); rdw(0, 0); mf(13, 1); rdw(0, 1); mf(13, 2); rdx(0, 0); mf(13, 3); rdx(0, 1);
        mf(14, 0); rdw(1, 0); mf(14, 1); rdw(1, 1); mf(14, 2); rdx(0, 2); mf(14, 3); rdx(0, 3);
        mf(15, 0); rdw(2, 0); mf(15, 1); rdw(2, 1); mf(15, 2); mf(15, 3);
        body(((s + 1) % 3 == 0 ? 0 : (s + 1) % 3 == 1 ? 2 : 1) * SLOT, false);
      }
      sb();
      asm volatile("" : "+v"(cacc[0]), "+v"(cacc[1]));
      if ((lane & 15) == 0) { *(LDS f32x4*)(size_t)(l0 + L_CS + w * 128 + g * 16) = cacc[0]; *(LDS f32x4*)(size_t)(l0 + L_CS + w * 128 + 64 + g * 16) = cacc[1]; }
      if (w < 3) { *(LDS u32x4*)(size_t)(l0 + L_KC + w * 1536 + lane * 16) = o01; *(LDS u32x2*)(size_t)(l0 + L_KC + w * 1536 + 1024 + lane * 8) = o2; }
      wait_lgkm0m();
      bar();
      #pragma unroll
      for (int n = 13; n < 16; n++) { mf(n, 0); mf(n, 1); mf(n, 2); mf(n, 3); }
      __builtin_amdgcn_s_setprio(2);
      ST(2);
      float nm[4], k0[4], k1[4], sq[4];
      #pragma unroll
      for (int t = 0; t < 4; t++) {
        unsigned st = *(LDS unsigned*)(size_t)(l0 + L_ST + (w * 64 + t * 16 + (lane & 15)) * 4);
        float den = bf_hi(st), ri = __builtin_amdgcn_rcpf(den);
        nm[t] = -bf_lo(st); k0[t] = -2.3022082f * ri; k1[t] = -2.3022082f * 0.044715f * ri * ri * ri; sq[t] = den * den;
      }
      #pragma unroll
      for (int f = 0; f < 8; f++) asm volatile("s_nop 0" : "+v"(acc[f][0]), "+v"(acc[f][1]), "+v"(acc[f][2]), "+v"(acc[f][3]));
      #pragma unroll
      for (int u = 0; u < 4; u++) {
        const int ug = (u + w) & 3;
        const f32x4 cg = *(LDS f32x4*)(size_t)(l0 + L_CS + ug * 128 + g * 16), cu = *(LDS f32x4*)(size_t)(l0 + L_CS + ug * 128 + 64 + g * 16);
        unsigned o[4][2];
        #pragma unroll
        for (int t = 0; t < 4; t++)
          #pragma unroll
          for (int e = 0; e < 2; e++) {
            f32x2 gg = f32x2{cg[2 * e], cg[2 * e + 1]} * nm[t] + f32x2{acc[2 * u][t][2 * e], acc[2 * u][t][2 * e + 1]};
            f32x2 uu = f32x2{cu[2 * e], cu[2 * e + 1]} * nm[t] + f32x2{acc[2 * u + 1][t][2 * e], acc[2 * u + 1][t][2 * e + 1]};
            f32x2 a = gg * (gg * gg * k1[t] + k0[t]);
            f32x2 ee = {__builtin_amdgcn_exp2f(a[0]), __builtin_amdgcn_exp2f(a[1])};
            ee = ee * sq[t] + sq[t];
            f32x2 hv = gg * uu * f32x2{__builtin_amdgcn_rcpf(ee[0]), __builtin_amdgcn_rcpf(ee[1])};
            o[t][e] = pk2(hv[0], hv[1]);
          }
        #pragma unroll
        for (int k = 0; k < 2; k++) {
          auto r0 = __builtin_amdgcn_permlane16_swap(o[2 * k][0], o[2 * k + 1][0], false, false), r1 = __builtin_amdgcn_permlane16_swap(o[2 * k][1], o[2 * k + 1][1], false, false);
          __builtin_amdgcn_raw_buffer_store_b128(u32x4{r0[0], r1[0], r0[1], r1[1]}, rw, O_HT + (((panel * 8 + w * 2 + k) * 108 + slice * 4 + ug) * 64 + lane) * 16, 0, SC1);
        }
      }
      bar();
      __builtin_amdgcn_s_setprio(0);
      ST(3);
      publish(ws, O_FH + ((panel * 4 + w) * 32 + slice) * FPS, tag, lane);
      ST(4);
    }
  } else {
    const int ai = xcd * 5 + r - 27, job = w * 40 + ai, job2 = 320 + (7 - w) * 40 + 39 - ai;
    const bool hasln = r < 31;
    const int tg = xcd * 4 + r - 27;
    const unsigned R = l0 + 81920 + w * 8192;
    unsigned xv[8][5];
    if (hasln) {
      const GAS unsigned* xp = (const GAS unsigned*)x + (tg * 8 + w) * 2560 + lane;
      #pragma unroll
      for (int i = 0; i < 8; i++)
        #pragma unroll
        for (int jj = 0; jj < 5; jj++) xv[i][jj] = xp[i * 320 + jj * 64];
    }
    rt_load(job, R);
    if (job2 < 540) rt_load(job2, R + 4096);
    if (hasln) layernorm(xv, tg * 8 + w, l0, rw, lane);
    ST(9);
    GET_TAG(0);
    rt_store(job, R);
    if (job2 < 540) rt_store(job2, R + 4096);
    if (w == 7) lds_put(l0 + L_FLAG, 0);
    wait_lgkm0m(); bar();
    if (hasln) {
      #pragma unroll
      for (int i = 0; i < 10; i++) {
        const int ps = (w * 10 + i) * 8 + (lane >> 3);
        __builtin_amdgcn_raw_buffer_store_b128(*(LDS u32x4*)(size_t)(l0 + ps * 128 + (((lane ^ ps) & 7) << 4)), rw, O_XNB + ps * TP + tg * 128 + (lane & 7) * 16, 0, SC1);
      }
    }
    ST(10);
    wait_vm<0>(); bar();
    if (w == 0 && lane == 0) __hip_atomic_store((GAS u64*)(ws + O_FA + ai * 8), tag, RLX);
    ST(2);
    if (w == 7) { __builtin_amdgcn_s_sleep(AUXS); poll(ws, O_FA + (lane < 40 ? lane : 0) * 8, tag); }
    bar();
    ST(11);
    if (w >= 4 && w < 7) { wd_to_lds(7 * (w - 4), w == 6 ? 20 : 7 * (w - 3)); wait_vm<0>(); lds_put(l0 + L_RDY + 4 * (w - 4), 1); wait_lgkm0m(); }
    bar();
    if (w == 7) g2_wave7(false);
  }

  if (w == 7) {
  } else if (w >= 4) {
    if (!gemm) load_x();
    if (w == 4) {
      auto prod = [&](int c) { return O_FS + (((c & ~127) + (c & 1) * 64 + ((c & 127) >> 1)) / 3) * FPS; };
      poll(ws, prod(cs * 80 + lane), prod(cs * 80 + 64 + (lane & 15)), tag);
      #pragma unroll
      for (int i = 0; i < 10; i++) glds1(l0 + L_S + i * 1024, (lane >> 3) * TP + (((lane & 7) ^ (2 * ((lane >> 4) & 3))) * 16), ws + O_ST + (size_t)(cs * 80 + i * 8) * TP + rt * 128);
    } else if (w == 5 && b == 255) {
      #pragma unroll 1
      for (int i = 0; i < 4; i++) { int t = i * 64 + lane; poll(ws, t < 216 ? O_FH + ((t / 27) * 128 + t % 27) * FPS : O_FA + (t - 216) * 8, tag); }
      if (lane == 0) __hip_atomic_store((GAS u64*)(ws + O_E), tag, RLX);
    }
    wait_vm<0>();
    ST(6);
  } else {
    int ks[12];
    #pragma unroll
    for (int i = 0; i < 12; i++) ks[i] = kidx(w, i);
    bf16x8 W[4][5], H[4][4];
    #pragma unroll
    for (int i = 0; i < 4; i++)
      #pragma unroll
      for (int c = 0; c < 5; c++) W[i][c] = ldWs(ks[i], c);
    sb();
    lds_wait(l0 + L_FLAG);
    ST(5);
    constexpr int ord[12] = {0, 4, 1, 5, 2, 6, 3, 7, 9, 8, 10, 11};
    constexpr int set[12] = {0, 0, 1, 1, 2, 2, 3, 3, 0, 0, 1, 2};
    #pragma unroll
    for (int i = 0; i < 3; i++)
      #pragma unroll
      for (int t = 0; t < 4; t++) H[i][t] = ldHs(ks[ord[i]], t);
    f32x4 acc[4][5];
    const unsigned wl = l0 + lane * 16 + (w < 2 ? w * 25600 : 98304 + (w * 5 - 11) * 5120), wl0 = w == 2 ? l0 + lane * 16 + 51200 : wl;
    #pragma unroll
    for (int i = 0; i < 12; i++) {
      const bool inl = ord[i] >= 4 && ord[i] < 9, nxl = i < 11 && ord[i + 1] >= 4 && ord[i + 1] < 9;
      #pragma unroll
      for (int c = 0; c < 5; c++) {
        #pragma unroll
        for (int t = 0; t < 4; t++) {
          const int tt = (c & 1) ? 3 - t : t;
          if (i == 0) mf16z(acc[tt][c], W[set[i]][c], H[i & 3][tt]); else mf16(acc[tt][c], W[set[i]][c], H[i & 3][tt]);
          if (t == 0 && c < 4 && i + 3 < 12) H[(i + 3) & 3][c] = ldHs(ks[ord[i + 3]], c);
        }
        if (nxl) W[set[i]][c] = *(LDS bf16x8*)((LDS char*)(size_t)(ord[i + 1] == 4 ? wl0 : wl) + (ord[i + 1] - 4) * 5120 + c * 1024);
        else if (inl && ord[i] < 7) W[set[i]][c] = ldWs(ks[ord[i] + 5], c);
      }
      if (i == XLD) load_x();
    }
    mf_pad();
    ST(6);
    put_acc(w == 2 ? l0 + wslot(11) : l0 + (w < 2 ? w * 25600 : wslot(15)), acc);
  }
  wait_lgkm0m();
  bar();
  ST(7);
  {
    const rsrc_t ro = __builtin_amdgcn_make_buffer_rsrc(out, 0, L * D * 2, 0x00020000);
    #pragma unroll
    for (int c = 0; c < 3; c++) {
      if (c == 2 && w >= 4) break;
      LDS char* a = (LDS char*)(size_t)(l0 + (tt_ * 5 + c0 + c) * 1024 + lane * 16);
      const f32x4 o = ((*(LDS f32x4*)a + *(LDS f32x4*)(a + 25600)) + (*(LDS f32x4*)(a + L_CONV) + (*(LDS f32x4*)(a + wslot(11)) + *(LDS f32x4*)(a + wslot(15)))));
      const int row = (c0 + c) * 16 + 4 * g + q;
      const u32x2 sv = __builtin_bit_cast(u32x2, trd(l0 + L_S + row * 128 + ((tt_ ^ ((row >> 1) & 3)) * 32) + 8 * p));
      u32x2 y;
      #pragma unroll
      for (int e = 0; e < 2; e++) {
        const f32x2 oo = {o[2 * e], o[2 * e + 1]}, a2 = oo * -1.4426950408889634f;
        const f32x2 d = f32x2{__builtin_amdgcn_exp2f(a2[0]), __builtin_amdgcn_exp2f(a2[1])} + 1.f;
        const f32x2 v = oo * f32x2{__builtin_amdgcn_rcpf(d[0]), __builtin_amdgcn_rcpf(d[1])} * f32x2{bf_lo(sv[e]), bf_hi(sv[e])} + f32x2{bf_lo(xr[c][e]), bf_hi(xr[c][e])};
        y[e] = pk2(v[0], v[1]);
      }
      __builtin_amdgcn_raw_buffer_store_b64(y, ro, ((rt * 64 + tt_ * 16 + (lane & 15)) * D + cs * 80 + (c0 + c) * 16 + g * 4) * 2, 0, OUTAUX);
    }
  }
  if (w != 7) ST(15);
  ST_DUMP();
}

extern "C" void kernel_launch(void* const* d_in, const int* in_sizes, int n_in, void* d_out, int out_size, void* d_ws, size_t ws_size, hipStream_t stream) {
  (void)hipFuncSetAttribute((const void*)fused_kernel, hipFuncAttributeMaxDynamicSharedMemorySize, LDS_BYTES);
  fused_kernel<<<256, 512, LDS_BYTES, stream>>>((const u16*)d_in[0], (const u16*)d_in[1], (const u16*)d_in[2], (const u16*)d_in[3], (const u16*)d_in[4], (const u16*)d_in[5],
      (const u16*)d_in[6], (const u16*)d_in[8], (const u16*)d_in[9], (const u16*)d_in[10], (u16*)d_out, (char*)d_ws);
}
```

```cpp
#include <hip/hip_runtime.h>
#include <stdint.h>

typedef unsigned short u16;
typedef unsigned long long u64;
typedef __attribute__((ext_vector_type(4))) short s16x4;
typedef __attribute__((ext_vector_type(8))) __bf16 bf16x8;
typedef __attribute__((ext_vector_type(2))) __bf16 bf16x2;
typedef __attribute__((ext_vector_type(2))) float f32x2;
typedef __attribute__((ext_vector_type(4))) float f32x4;
typedef __attribute__((ext_vector_type(16))) float f32x16;
typedef __attribute__((ext_vector_type(2))) unsigned u32x2;
typedef __attribute__((ext_vector_type(4))) unsigned u32x4;
#define LDS __attribute__((address_space(3)))
#define GAS __attribute__((address_space(1)))
#define RLX __ATOMIC_RELAXED, __HIP_MEMORY_SCOPE_AGENT
#define SC1 16

constexpr int L = 2048, D = 640, F = 1728;
#define FPS 128
constexpr unsigned O_E = 0, O_FA = 1024, O_FH = 8192  , O_FS = O_FH + 1024 * 128;
constexpr unsigned O_STAT = 524288, O_XNB = 1u << 20, O_WDT = 4u << 20, O_KX = 7u << 20, O_HT = 13u << 20, O_ST = 21u << 20, WS_BYTES = 24u << 20;
constexpr unsigned KXB = 8448, KCB = 1536;
constexpr unsigned TP = 4224;
#define DI __device__ __forceinline__
constexpr unsigned SLOT = 49152;
DI constexpr unsigned wslot(int i) { return i < 11 ? i * 5120 : 98304 + (i - 11) * 5120; }
constexpr unsigned L_CONV = 59136, CONVB = 13056;
constexpr unsigned L_S = 147456;
constexpr unsigned L_KC = L_S;
constexpr unsigned L_CS = 159744;
constexpr unsigned L_ST = 160256;
constexpr unsigned L_FULL = 161280, L_FREE = L_FULL + 16, L_RDY = L_FULL + 32, L_FLAG = L_FULL + 48  , L_DUMP = L_FULL + 256;
constexpr unsigned LDS_BYTES = 163840;
constexpr unsigned AUXW = 20480;
#define SPL 20000
constexpr int SPIN_LIMIT = SPL;

#define ST(i) do {} while (0)
#define ST_INIT() do {} while (0)
#define ST_DUMP() do {} while (0)

DI float bf_lo(unsigned v) { return __uint_as_float(v << 16); }
DI float bf_hi(unsigned v) { return __uint_as_float(v & 0xffff0000u); }
DI unsigned pk2(float a, float b) { return __builtin_bit_cast(unsigned, __builtin_convertvector(f32x2{a, b}, bf16x2)); }
DI float rbf(float a) { return bf_lo(pk2(a, a) & 0xffffu); }

DI void glds(unsigned lds, unsigned voff, const char* sbase) {
  asm volatile("s_mov_b32 m0, %0\n\ts_nop 0\n\tglobal_load_lds_dwordx4 %1, %2" :: "s"(lds), "v"(voff), "s"(sbase) : "memory");
}
DI void glds1(unsigned lds, unsigned voff, const char* sbase) {
  asm volatile("s_mov_b32 m0, %0\n\ts_nop 0\n\tglobal_load_lds_dwordx4 %1, %2 sc1" :: "s"(lds), "v"(voff), "s"(sbase) : "memory");
}
DI void set_m0(unsigned lds) { asm volatile("s_mov_b32 m0, %0\n\ts_nop 0" :: "s"(lds) : "memory"); }
template<int O> DI void gl(unsigned voff, const char* sbase) { asm volatile("global_load_lds_dwordx4 %0, %1 offset:%2" :: "v"(voff), "s"(sbase), "i"(O) : "memory"); }
template<int O> DI void gl1(unsigned voff, const char* sbase) { asm volatile("global_load_lds_dwordx4 %0, %1 offset:%2 sc1" :: "v"(voff), "s"(sbase), "i"(O) : "memory"); }
DI void touch(unsigned lds, const char* p) { asm volatile("s_mov_b32 m0, %0\n\ts_nop 0\n\tglobal_load_lds_dword %1, off" :: "s"(lds), "v"(p) : "memory"); }
DI void touch1(unsigned lds, unsigned voff, const char* sbase) { asm volatile("s_mov_b32 m0, %0\n\ts_nop 0\n\tglobal_load_lds_dword %1, %2 sc1" :: "s"(lds), "v"(voff), "s"(sbase) : "memory"); }
template<int N> DI void wait_vm() { asm volatile("s_waitcnt vmcnt(%0)" :: "i"(N) : "memory"); }
template<int B> DI void wait_vmx(int n) {
  if (n == 0) wait_vm<B>();
  else if (n < 3) { if (n == 1) wait_vm<B + 1>(); else wait_vm<B + 2>(); }
  else if (n < 5) { if (n == 3) wait_vm<B + 3>(); else wait_vm<B + 4>(); }
  else if (n == 5) wait_vm<B + 5>(); else wait_vm<B + 6>();
}
DI void sb() { __builtin_amdgcn_sched_barrier(0); }
DI void bar() { sb(); __builtin_amdgcn_s_barrier(); sb(); }
DI void wait_lgkm0() { __builtin_amdgcn_s_waitcnt(0xC07F); }
DI void wait_lgkm0m() { asm volatile("s_waitcnt lgkmcnt(0)" ::: "memory"); }
DI s16x4 trd(unsigned a) { return __builtin_amdgcn_ds_read_tr16_b64_v4i16((LDS s16x4*)(size_t)a); }
DI bf16x8 tr2(unsigned a, unsigned b) { return __builtin_bit_cast(bf16x8, __builtin_shufflevector(trd(a), trd(b), 0, 1, 2, 3, 4, 5, 6, 7)); }
DI bf16x8 ld128(unsigned a) { return *(LDS bf16x8*)(size_t)a; }
#define MFMA32(a, b, c) __builtin_amdgcn_mfma_f32_32x32x16_bf16(a, b, c, 0, 0, 0)
#define MFMA16(a, b, c) __builtin_amdgcn_mfma_f32_16x16x32_bf16(a, b, c, 0, 0, 0)
#define SGB(m, n) __builtin_amdgcn_sched_group_barrier(m, n, 0)
typedef __amdgpu_buffer_rsrc_t rsrc_t;
DI void mf16(f32x4& c, const bf16x8& a, const bf16x8& b) { asm volatile("v_mfma_f32_16x16x32_bf16 %0, %1, %2, %0" : "+v"(c) : "v"(a), "v"(b)); }
DI void mf16z(f32x4& c, const bf16x8& a, const bf16x8& b) { asm volatile("v_mfma_f32_16x16x32_bf16 %0, %1, %2, 0" : "=&v"(c) : "v"(a), "v"(b)); }
DI void mf_pad() { asm volatile("s_nop 7\n\ts_nop 1" ::: "memory"); }
typedef u32x2 __attribute__((aligned(4))) u32x2a;

DI void poll(const char* ws, unsigned off, unsigned off2, u64 tag) {
  #pragma unroll 1
  for (int it = 0; it < SPIN_LIMIT; it++) {
    const u64 v = __hip_atomic_load((const GAS u64*)(ws + off), RLX), v2 = __hip_atomic_load((const GAS u64*)(ws + off2), RLX);
    if (__all(v == tag && v2 == tag)) break;
    if (__any(v == tag)) __builtin_amdgcn_s_sleep(2); else __builtin_amdgcn_s_sleep(12);
  }
  asm volatile("" ::: "memory");
}
DI void poll(const char* ws, unsigned off, u64 tag) {
  #pragma unroll 1
  for (int it = 0; it < SPIN_LIMIT; it++) {
    const u64 v = __hip_atomic_load((const GAS u64*)(ws + off), RLX);
    if (__all(v == tag)) break;
    if (__any(v == tag)) __builtin_amdgcn_s_sleep(2); else __builtin_amdgcn_s_sleep(12);
  }
  asm volatile("" ::: "memory");
}
DI void publish(char* ws, unsigned off, u64 tag, int lane) {
  asm volatile("s_waitcnt vmcnt(0)" ::: "memory");
  if (lane == 0) __hip_atomic_store((GAS u64*)(ws + off), tag, RLX);
}
DI void lds_wait(unsigned a) {
  #pragma unroll 1
  for (int it = 0; it < 320000; it++) {
    if (__builtin_amdgcn_readfirstlane(*(volatile LDS unsigned*)(size_t)a)) break;
    __builtin_amdgcn_s_sleep(1);
  }
  asm volatile("" ::: "memory");
}
DI unsigned min4(u32x4 v) { return __builtin_amdgcn_readfirstlane(min(min(v[0], v[1]), min(v[2], v[3]))); }
DI unsigned lds_min4(unsigned a) { return min4(*(volatile LDS u32x4*)(size_t)a); }
template<int SL> DI void lds_wait4(unsigned a, unsigned need) {
  #pragma unroll 1
  for (int it = 0; it < 320000 && lds_min4(a) < need; it++) __builtin_amdgcn_s_sleep(SL);
  asm volatile("" ::: "memory");
}
DI void lds_put(unsigned a, unsigned v) { if ((threadIdx.x & 63) == 0) *(volatile LDS unsigned*)(size_t)a = v; }
DI int pos2ch(int pos) { return (pos & ~127) + 2 * (pos & 63) + ((pos >> 6) & 1); }

DI float red8(const float (&a)[8], int lane) {
  float b[4], c[2];
  #pragma unroll
  for (int i = 0; i < 4; i++) { auto r = __builtin_amdgcn_permlane32_swap(__float_as_uint(a[2 * i]), __float_as_uint(a[2 * i + 1]), false, false); b[i] = __uint_as_float(r[0]) + __uint_as_float(r[1]); }
  #pragma unroll
  for (int i = 0; i < 2; i++) { auto r = __builtin_amdgcn_permlane16_swap(__float_as_uint(b[2 * i]), __float_as_uint(b[2 * i + 1]), false, false); c[i] = __uint_as_float(r[0]) + __uint_as_float(r[1]); }
  bool up = lane & 8;
  float keep = up ? c[1] : c[0], send = up ? c[0] : c[1];
  float d = keep + __uint_as_float(__builtin_amdgcn_update_dpp(0, __float_as_uint(send), 0x128, 0xf, 0xf, true));
  d += __uint_as_float(__builtin_amdgcn_update_dpp(0, __float_as_uint(d), 0x141, 0xf, 0xf, true));
  d += __uint_as_float(__builtin_amdgcn_update_dpp(0, __float_as_uint(d), 0xB1, 0xf, 0xf, true));
  d += __uint_as_float(__builtin_amdgcn_update_dpp(0, __float_as_uint(d), 0x4E, 0xf, 0xf, true));
  return d;
}
DI constexpr int red8_lane(int i) { return ((i & 1) << 5) | (((i >> 1) & 1) << 4) | (((i >> 2) & 1) << 3); }
DI float rdl(float v, int l) { return __uint_as_float(__builtin_amdgcn_readlane(__float_as_uint(v), l)); }

DI f32x2 cmul(f32x2 a, f32x2 b) {
  f32x2 r;
  asm("v_pk_mul_f32 %0, %1, %2 op_sel:[1,1] op_sel_hi:[1,0] neg_lo:[0,1]\n\tv_pk_fma_f32 %0, %1, %2, %0 op_sel_hi:[0,1,1]" : "=&v"(r) : "v"(a), "v"(b));
  return r;
}
DI f32x2 zpow(float al, float be, float p) {
  float e = __builtin_amdgcn_exp2f(al * p), f = __builtin_amdgcn_fractf(be * p);
  return f32x2{e * __builtin_amdgcn_cosf(f), e * __builtin_amdgcn_sinf(f)};
}

typedef __attribute__((ext_vector_type(2))) _Float16 h2;
typedef __attribute__((ext_vector_type(8))) _Float16 f16x8;
DI h2 cmulh(h2 a, h2 b) { h2 t = h2{a[1], a[1]} * h2{-b[1], b[0]}; return __builtin_elementwise_fma(h2{a[0], a[0]}, b, t); }
DI h2 toh(f32x2 a) { return __builtin_convertvector(a, h2); }
DI h2 tohc(f32x2 a) { h2 r; asm("v_cvt_pk_f16_f32 %0, %1, -%2" : "=v"(r) : "v"(a[0]), "v"(a[1])); return r; }
template<int C> DI float maxd(float v) { return fmaxf(v, __uint_as_float(__builtin_amdgcn_update_dpp(0, __float_as_uint(v), C, 0xf, 0xf, true))); }

struct KPar { unsigned lar, aim, cre, cim; unsigned ldt; };
DI KPar kpar_load(const u16* lar, const u16* aim, const u16* cre, const u16* cim, const u16* ldt, int ch, int lane) {
  KPar p; int o = ch * 64 + lane;
  p.lar = ((const GAS unsigned*)lar)[o]; p.aim = ((const GAS unsigned*)aim)[o]; p.cre = ((const GAS unsigned*)cre)[o]; p.cim = ((const GAS unsigned*)cim)[o];
  p.ldt = ((const GAS u16*)ldt)[ch];
  return p;
}
DI void kgen(const KPar& P, unsigned S0, unsigned S1, u32x4& o01, u32x2& o2, int lane) {
  const float LOG2E = 1.4426950408889634f;
  const float dt = rbf(__builtin_amdgcn_exp2f(bf_lo(P.ldt) * LOG2E));
  f32x2 C[2] = {f32x2{bf_lo(P.cre), bf_lo(P.cim)}, f32x2{bf_hi(P.cre), bf_hi(P.cim)}};
  float mx = fmaxf(fmaxf(fabsf(C[0][0]), fabsf(C[0][1])), fmaxf(fabsf(C[1][0]), fabsf(C[1][1])));
  mx = maxd<0xB1>(mx); mx = maxd<0x4E>(mx); mx = maxd<0x141>(mx); mx = maxd<0x140>(mx);
  mx = fmaxf(fmaxf(rdl(mx, 0), rdl(mx, 16)), fmaxf(rdl(mx, 32), rdl(mx, 48)));
  const int ex = __builtin_amdgcn_frexp_expf(mx);
  const float cs = __builtin_amdgcn_ldexpf(1.f, -ex), ks = __builtin_amdgcn_ldexpf(2.f * dt, ex);
  h2 T[2][8], Pw[2][4], Q[2][4], U[2][17];
  #pragma unroll
  for (int m = 0; m < 2; m++) {
    float la = m ? bf_hi(P.lar) : bf_lo(P.lar), ai = m ? bf_hi(P.aim) : bf_lo(P.aim);
    float al = -rbf(__builtin_amdgcn_exp2f(la * LOG2E)) * dt * LOG2E, be = ai * dt * 0.15915494309189535f;
    f32x2 z1 = zpow(al, be, 1.f), u1 = zpow(al, be, 128.f);
    f32x2 z2 = cmul(z1, z1), z3 = cmul(z2, z1), z4 = cmul(z2, z2), z8 = cmul(z4, z4), z16 = cmul(z8, z8), z24 = cmul(z16, z8), z32 = cmul(z16, z16);
    f32x2 q0 = C[m] * cs, q1 = cmul(q0, z32), q2 = cmul(q1, z32), q3 = cmul(q2, z32);
    f32x2 u2 = cmul(u1, u1), u3 = cmul(u2, u1), u4 = cmul(u2, u2), u8 = cmul(u4, u4), u12 = cmul(u8, u4), u16 = cmul(u8, u8);
    T[m][0] = h2{1, 0}; T[m][1] = toh(z1); T[m][2] = toh(z2); T[m][3] = toh(z3); T[m][4] = toh(z4);
    Pw[m][1] = tohc(z8); Pw[m][2] = tohc(z16); Pw[m][3] = tohc(z24);
    Q[m][0] = tohc(q0); Q[m][1] = tohc(q1); Q[m][2] = tohc(q2); Q[m][3] = tohc(q3);
    U[m][1] = toh(u1); U[m][2] = toh(u2); U[m][3] = toh(u3); U[m][4] = toh(u4); U[m][8] = toh(u8); U[m][12] = toh(u12); U[m][16] = toh(u16);
  }
  const unsigned wa[2] = {S0 + lane * 8, S1 + lane * 8}, ra[2] = {S0 + (lane & 15) * 528 + (lane >> 4) * 16, S1 + (lane & 15) * 528 + (lane >> 4) * 16};
  auto put2 = [&](int buf, int row, h2 a0, h2 a1, h2 b0, h2 b1) {
    asm volatile("ds_write2_b64 %0, %1, %2 offset0:%3 offset1:%4" :: "v"(wa[buf] + (row & ~3) * 528), "v"(u32x2{__builtin_bit_cast(unsigned, a0), __builtin_bit_cast(unsigned, a1)}),
                 "v"(u32x2{__builtin_bit_cast(unsigned, b0), __builtin_bit_cast(unsigned, b1)}), "i"((row & 3) * 66), "i"((row & 3) * 66 + 66) : "memory");
  };
  auto frag = [&](int buf, int k) { return *(LDS f16x8*)((LDS char*)(size_t)ra[buf] + k * 64); };
  #define CM(X, i, Y, j) cmulh(X[0][i], Y[0][j]), cmulh(X[1][i], Y[1][j])
  #define ID(X, i) X[0][i], X[1][i]
  #pragma unroll
  for (int a = 0; a < 4; a++) { put2(0, 4 * a, ID(Q, a), CM(Q, a, Pw, 1)); put2(0, 4 * a + 2, CM(Q, a, Pw, 2), CM(Q, a, Pw, 3)); }
  #pragma unroll
  for (int e = 5; e < 8; e++) { T[0][e] = cmulh(T[0][4], T[0][e - 4]); T[1][e] = cmulh(T[1][4], T[1][e - 4]); }
  #pragma unroll
  for (int e = 0; e < 8; e += 2) put2(1, e, ID(T, e), ID(T, e + 1));
  f16x8 fine[8], cf[8];
  #pragma unroll
  for (int k = 0; k < 8; k++) fine[k] = frag(0, k);
  put2(1, 8, ID(U, 1), CM(T, 1, U, 1));
  #pragma unroll
  for (int e = 2; e < 8; e += 2) put2(1, 8 + e, CM(T, e, U, 1), CM(T, e + 1, U, 1));
  #pragma unroll
  for (int a = 2; a < 4; a++) { put2(0, 4 * (a - 2), ID(U, a), CM(T, 2, U, a)); put2(0, 4 * (a - 2) + 2, CM(T, 4, U, a), CM(T, 6, U, a)); }
  #pragma unroll
  for (int k = 0; k < 8; k++) cf[k] = frag(1, k);
  f32x4 acc[3][2];
  auto mm = [&](int t, int k) { acc[t][k & 1] = __builtin_amdgcn_mfma_f32_16x16x32_f16(cf[k], fine[k], k < 2 ? f32x4{0, 0, 0, 0} : acc[t][k & 1], 0, 0, 0); };
  #pragma unroll
  for (int a = 4; a < 8; a++) {
    if (a > 4) { U[0][a] = cmulh(U[0][4], U[0][a - 4]); U[1][a] = cmulh(U[1][4], U[1][a - 4]); }
    put2(0, 8 + 2 * (a - 4), ID(U, a), CM(T, 4, U, a));
    mm(0, 2 * (a - 4)); mm(0, 2 * (a - 4) + 1);
  }
  #pragma unroll
  for (int k = 0; k < 8; k++) cf[k] = frag(0, k);
  #pragma unroll
  for (int a = 8; a < 16; a += 4) {
    put2(1, a - 8, ID(U, a), CM(U, a, U, 1)); mm(1, a - 8); mm(1, a - 7);
    put2(1, a - 6, CM(U, a, U, 2), CM(U, a, U, 3)); mm(1, a - 6); mm(1, a - 5);
  }
  put2(1, 8, ID(U, 16), ID(U, 16));
  #pragma unroll
  for (int k = 0; k < 8; k++) cf[k] = frag(1, k);
  const f32x4 k0 = (acc[0][0] + acc[0][1]) * ks, k1 = (acc[1][0] + acc[1][1]) * ks;
  o01 = u32x4{pk2(k0[0], k0[1]), pk2(k0[2], k0[3]), pk2(k1[0], k1[1]), pk2(k1[2], k1[3])};
  #pragma unroll
  for (int k = 0; k < 8; k++) mm(2, k);
  const f32x4 k2 = (acc[2][0] + acc[2][1]) * ks;
  o2 = u32x2{pk2(k2[0], k2[1]), pk2(k2[2], k2[3])};
  #undef CM
  #undef ID
}
DI unsigned prm(unsigned hi, unsigned lo, unsigned sel) { return __builtin_amdgcn_perm(hi, lo, sel); }
DI void kexpand(u32x4 k01, u32x2 k2, unsigned S, int lane) {
  const int b = lane & 15, g = lane >> 4;
  const unsigned TR = S + 4608 + lane * 16;
  const unsigned LL = 0x01000100u, HH = 0x03020302u;
  unsigned a3 = g < 2 ? S + 8 + 2 * (2044 - (1024 + 512 * g + 384 + 8 * b)) : TR;
  unsigned a4 = (g < 2 && !(b & 1)) ? a3 - 2 : TR;
  unsigned v3[4];
  #pragma unroll
  for (int j = 0; j < 4; j++) { v3[j] = prm(0, k2[j >> 1], (j & 1) ? HH : LL); *(LDS u32x4*)((LDS char*)(size_t)a3 + 256 * (3 - j)) = u32x4{v3[j], v3[j], v3[j], v3[j]}; }
  *(LDS u32x4*)(size_t)(lane == 32 ? S : TR) = u32x4{v3[0], v3[0], v3[0], v3[0]};
  #pragma unroll
  for (int j = 0; j < 4; j++) *(volatile LDS u16*)((LDS char*)(size_t)a4 + 256 * (3 - j)) = (u16)v3[j];
  unsigned a2 = g >= 2 ? S + 8 + 2 * (2042 - (512 + 256 * (g - 2) + 128 + 8 * b)) : TR;
  const unsigned l0_ = prm(0, k01[2], LL), h0_ = prm(0, k01[2], HH), l1_ = prm(0, k01[3], LL), h1_ = prm(0, k01[3], HH);
  #pragma unroll
  for (int h = 0; h < 2; h++) {
    volatile LDS unsigned* q = (volatile LDS unsigned*)((LDS char*)(size_t)a2 + 256 * (1 - h));
    q[0] = h ? h1_ : h0_; q[1] = prm(0, k01[2 + h], 0x01000302u); q[2] = h ? l1_ : l0_; q[3] = h ? l1_ : l0_;
  }
  LDS char* a1 = (LDS char*)(size_t)(g < 2 ? S + 8 + 2 * (2041 - (256 + 128 * g + 8 * b)) : TR + 2);
  *(volatile LDS u16*)a1 = (u16)h1_;
  *(volatile LDS unsigned*)(a1 + 2) = l1_;
  *(volatile LDS unsigned*)(a1 + 6) = prm(k01[2], k01[3], 0x07060100u);
  *(volatile LDS unsigned*)(a1 + 10) = l0_;
  *(volatile LDS u16*)(a1 + 14) = (u16)l0_;
  unsigned a0 = S + 8 + 2 * (2044 - (128 * (g >> 1) + 4 * (g & 1) + 8 * b));
  *(volatile LDS u32x2*)(size_t)a0 = u32x2{prm(0, k01[1], 0x01000302u), prm(0, k01[0], 0x01000302u)};
  *(volatile LDS u32x2*)(size_t)(lane < 6 ? S + 8 + 4096 + 8 * lane : TR) = u32x2{0, 0};
  asm volatile("" ::: "memory");
  u32x2 x[5], y[5]; unsigned z[5];
  #pragma unroll
  for (int i = 0; i < 5; i++) {
    LDS char* a = (LDS char*)(size_t)(S + 8 + 16 * (i < 4 ? lane : (lane & 1))) + 1024 * i;
    x[i] = *(LDS u32x2*)a; y[i] = *(LDS u32x2*)(a + 8); z[i] = *(LDS unsigned*)(a + 16);
  }
  asm volatile("" ::: "memory");
  #pragma unroll
  for (int i = 0; i < 5; i++) {
    LDS char* a = (LDS char*)(size_t)(S + 4168 + 16 * (i < 4 ? lane : (lane & 1))) + 1024 * i;
    if (i < 4 || lane < 2) {
      *(LDS u32x2*)a = u32x2{__builtin_amdgcn_alignbit(x[i][1], x[i][0], 16), __builtin_amdgcn_alignbit(y[i][0], x[i][1], 16)};
      *(LDS u32x2*)(a + 8) = u32x2{__builtin_amdgcn_alignbit(y[i][1], y[i][0], 16), __builtin_amdgcn_alignbit(z[i], y[i][1], 16)};
    }
  }
  asm volatile("" ::: "memory");
}

#define CONVU 1
constexpr int CONV_U = CONVU;
DI void conv(unsigned KB, unsigned UB, float dsk, rsrc_t rw, unsigned goff, int lane) {
  const int r = lane & 15, kg = lane >> 4;
  const int n0 = 2047 - (16 * 112 + 16 * (kg >> 1) + r - 8 * (kg & 1));
  unsigned ka = KB + 8 + ((n0 & 1) ? 4160 + 2 * (n0 - 1) : 2 * n0);
  unsigned ua = UB + 2 * (256 + 16 * r - 16 * (kg >> 1) + 8 * (kg & 1));
  f32x4 acc[8];
  #pragma unroll
  for (int t = 0; t < 8; t++) { acc[t] = f32x4{0, 0, 0, 0}; asm volatile("" : "+v"(acc[t])); }
  u32x2 A[2][8][2]; bf16x8 Fr[2][8];
  unsigned kb[4];
  #pragma unroll
  for (int i = 0; i < 4; i++) kb[i] = ka + (3 - i) * 1024 - 64;
  ua -= 64;
  auto ld1 = [&](int s, int n, int e) {
    const int q = n / 3;
    if (n % 3 < 2) A[s][q][n % 3] = *(LDS u32x2a*)((LDS char*)(size_t)kb[q >> 1] + ((q & 1) ? 0 : 512) + e + 8 * (n % 3));
    else Fr[s][q] = *(LDS bf16x8*)((LDS char*)(size_t)ua + q * 512 + e);
  };
  auto dec = [&](unsigned& v) { asm volatile("v_subrev_u32 %0, %1, %0" : "+v"(v) : "s"(128u)); };
  auto step = [&](int s, bool nxt) {
    int n = 0;
    #pragma unroll
    for (int q = 0; q < 8; q++)
      #pragma unroll
      for (int m = 7 - q; m >= 0; m--) {
        mf16(acc[q + m], __builtin_bit_cast(bf16x8, __builtin_shufflevector(A[s][q][0], A[s][q][1], 0, 1, 2, 3)), Fr[s][m]);
        if (n < 24 && nxt) ld1(s ^ 1, n, s ? 64 : 0);
        if (s == 0 && n >= 31) dec(n == 35 ? ua : kb[n - 31]);
        n++;
      }
  };
  #pragma unroll
  for (int i = 0; i < 4; i++) asm volatile("" : "+v"(kb[i]));
  asm volatile("" : "+v"(ua));
  #pragma unroll
  for (int n = 0; n < 24; n++) ld1(0, n, 64);
  #pragma unroll CONV_U
  for (int it = 0; it < 3; it++) { step(0, true); step(1, true); }
  step(0, true);
  step(1, false);
  mf_pad();
  const unsigned ue = UB + 2 * (256 + 16 * r + 4 * kg);
  #pragma unroll
  for (int t = 0; t < 8; t++) {
    u32x2 u = *(LDS u32x2*)(size_t)(ue + 512 * t);
    u32x2 o = {pk2(fmaf(bf_lo(u[0]), dsk, acc[t][0]), fmaf(bf_hi(u[0]), dsk, acc[t][1])), pk2(fmaf(bf_lo(u[1]), dsk, acc[t][2]), fmaf(bf_hi(u[1]), dsk, acc[t][3]))};
    __builtin_amdgcn_raw_buffer_store_b64(o, rw, goff + 512 * t + 32 * r + 8 * kg, 0, SC1);
  }
}

DI void layernorm(const unsigned (&xv)[8][5], int tb, unsigned img, rsrc_t rw, int lane) {
      float s8[8];
      const bf16x2 one2 = __builtin_bit_cast(bf16x2, 0x3f803f80u);
      #pragma unroll
      for (int i = 0; i < 8; i++) { float s = 0;
        #pragma unroll
        for (int jj = 0; jj < 5; jj++) s = __builtin_amdgcn_fdot2_f32_bf16(__builtin_bit_cast(bf16x2, xv[i][jj]), one2, s, false);
        s8[i] = s; }
      const float mean = red8(s8, lane) * (1.f / D);
      f32x2 dv[8][5];
      #pragma unroll
      for (int i = 0; i < 8; i++) {
        const float mi = rdl(mean, red8_lane(i));
        f32x2 qq = {0, 0};
        #pragma unroll
        for (int jj = 0; jj < 5; jj++) { dv[i][jj] = f32x2{bf_lo(xv[i][jj]), bf_hi(xv[i][jj])} - mi; qq += dv[i][jj] * dv[i][jj]; }
        s8[i] = qq[0] + qq[1];
      }
      const float var = rbf(red8(s8, lane) * (1.f / D));
      const float den = rbf(__builtin_sqrtf(rbf(var + 1e-5f))), mb = rbf(mean);
      const float ri = 1.f / den, off = (mean - mb) * ri;
      if ((lane & 7) == 0) __builtin_amdgcn_raw_buffer_store_b32(pk2(mb, den), rw, O_STAT + (tb * 8 + ((lane >> 5) | (((lane >> 4) & 1) << 1) | (((lane >> 3) & 1) << 2))) * 4, 0, SC1);
      #pragma unroll
      for (int i = 0; i < 8; i++) {
        const float rr = rdl(ri, red8_lane(i)), oo = rdl(off, red8_lane(i));
        #pragma unroll
        for (int jj = 0; jj < 5; jj++) dv[i][jj] = dv[i][jj] * rr + oo;
      }
      #pragma unroll
      for (int jj = 0; jj < 5; jj++)
        #pragma unroll
        for (int e = 0; e < 2; e++)
          *(LDS u32x4*)((LDS char*)(size_t)(img + lane * 128 + (((tb ^ lane) & 7) << 4)) + (jj * 128 + e * 64) * 128) =
              u32x4{pk2(dv[0][jj][e], dv[1][jj][e]), pk2(dv[2][jj][e], dv[3][jj][e]), pk2(dv[4][jj][e], dv[5][jj][e]), pk2(dv[6][jj][e], dv[7][jj][e])};
}

#define CODEPF 1
#define CODER 31
constexpr int CODEKB = 42;
__global__ __launch_bounds__(512, 2) void fused_kernel(const u16* x, const u16* lar, const u16* aim, const u16* cre,
    const u16* cim, const u16* ldt, const u16* dsk, const u16* wg, const u16* wu, const u16* wd,
    u16* out, char* ws) {
  extern __shared__ __attribute__((aligned(16))) char smem[];
  const int b = blockIdx.x, xcd = b & 7, r = b >> 3;
  const int w = __builtin_amdgcn_readfirstlane(threadIdx.x >> 6), lane = threadIdx.x & 63;
  const unsigned l0 = (unsigned)(size_t)(LDS char*)smem;
  asm volatile("" : "+s"(x), "+s"(wg), "+s"(wu), "+s"(wd), "+s"(ws), "+s"(out), "+s"(dsk), "+s"(lar), "+s"(aim), "+s"(cre), "+s"(cim), "+s"(ldt));
  const rsrc_t rw = __builtin_amdgcn_make_buffer_rsrc(ws, 0, WS_BYTES, 0x00020000);
  ST_INIT(); ST(0);
  if (r == CODER && w < (CODEKB + 7) / 8) {
    const char* pc; asm volatile("s_getpc_b64 %0" : "=s"(pc));
    touch(l0 + L_DUMP, pc + min(w * 64 + lane, CODEKB * 8 - 1) * 128);
  }
  const bool gemm = r < 27;
  const int gi = xcd * 27 + r;
  const int id4 = (xcd & 3) * 27 + r, slice = id4 >> 2, panel = (xcd >> 2) * 4 + (id4 & 3);
#define AUXT 1
#define WDE 1
#define OUTAUX 2
#define AUXS 40
#define XLD 8
#define PFM 1
#define PFD 15
#define G2MAP 0
  const int rt = G2MAP ? (xcd >> 1) * 8 + (r >> 2) : xcd * 4 + (r >> 3), cs = G2MAP ? (xcd & 1) * 4 + (r & 3) : r & 7;
  u32x2 e0;
  asm volatile("global_load_dwordx2 %0, %1, %2 sc1" : "=&v"(e0) : "v"(0u), "s"(ws + O_E) : "memory");
  #define GET_TAG(N) do { unsigned lo_, hi_; asm volatile("s_waitcnt vmcnt(" #N ")\n\tv_readfirstlane_b32 %0, %2\n\tv_readfirstlane_b32 %1, %3" : "=&s"(lo_), "=&s"(hi_) : "v"(e0[0]), "v"(e0[1]) : "memory"); \
    tag = (((u64)hi_ << 32) | lo_) * 6364136223846793005ull + 1442695040888963407ull; } while (0)
  u64 tag;

  const int rot = G2MAP ? ((rt & 7) * 3 + (cs & 3) * 6) >> 1 : (cs * 3 + (rt & 3) * 6) >> 1;
  auto kidx = [&](int wv, int i) { int v = i + rot; v -= v >= 24 ? 24 : v >= 12 ? 12 : 0; return 12 * wv + v; };
  auto kidx7 = [&](int i) { return 48 + (i + rot) % 6; };
  auto wd_to_lds = [&](int i0, int i1) {
    #pragma unroll 1
    for (int i = i0; i < i1; i++) {
      const int cw = (i * 13) >> 6;
      const char* src = ws + O_WDT + 2048 + (kidx(cw, 4 + i - cw * 5) * 40 + cs * 5) * 1024;
      set_m0(l0 + 2048 + i * 5120 + (i < 11 ? 0 : 98304 - 56320));
      gl1<-2048>(lane * 16, src); gl1<-1024>(lane * 16, src); gl1<0>(lane * 16, src); gl1<1024>(lane * 16, src); gl1<2048>(lane * 16, src);
    }
  };
  const unsigned wvo = O_WDT + (cs * 5 * 64 + lane) * 16;
  auto ldWs = [&](int s, int c) { return __builtin_bit_cast(bf16x8, __builtin_amdgcn_raw_buffer_load_b128(rw, wvo + c * 1024, s * 40960, SC1)); };
  const int g = lane >> 4, q = (lane & 15) >> 2, p = lane & 3;
  const unsigned hb = O_HT + ((rt * 2 * 108 + (lane >> 5)) * 64 + ((lane >> 4) & 1) * 32 + (lane & 15)) * 16;
  auto ldHs = [&](int s, int tt) { return __builtin_bit_cast(bf16x8, __builtin_amdgcn_raw_buffer_load_b128(rw, hb + (tt & 1) * 256, s * 2048 + (tt >> 1) * 110592, SC1)); };
  const int tt_ = w & 3, c0 = w < 4 ? 0 : 3;
  u32x2 xr[3];
  auto load_x = [&]() {
    #pragma unroll
    for (int c = 0; c < 3; c++) xr[c] = *(const GAS u32x2*)(x + (size_t)(rt * 64 + tt_ * 16 + (lane & 15)) * D + cs * 80 + min(c0 + c, 4) * 16 + g * 4);
  };
  auto put_acc = [&](unsigned a, const f32x4 (&acc)[4][5]) {
    #pragma unroll
    for (int t = 0; t < 4; t++)
      #pragma unroll
      for (int c = 0; c < 5; c++) *(LDS f32x4*)((LDS char*)(size_t)(a + lane * 16) + (t * 5 + c) * 1024) = acc[t][c];
  };
  auto rt_load = [&](int id, unsigned R) {
    const char* rb = (const char*)wd + (id / 10) * 40960 + (id % 10) * 128;
    const unsigned o = (lane >> 3) * 1280 + (((lane & 7) ^ (2 * ((lane >> 4) & 1))) << 4);
    set_m0(R);
    gl<0>(o, rb); gl<1024>((o ^ 64) + 9216, rb); gl<2048>(o + 18432, rb); gl<3072>((o ^ 64) + 27648, rb);
  };
  auto rt_store = [&](int id, unsigned R) {
    #pragma unroll
    for (int c = 0; c < 4; c++) {
      const unsigned a = R + (8 * g + q) * 128 + ((c ^ ((q >> 1) | ((g & 1) << 1))) * 32) + 8 * p;
      __builtin_amdgcn_raw_buffer_store_b128(__builtin_bit_cast(u32x4, tr2(a, a + 512)), rw, O_WDT + (((id / 10) * 40 + (id % 10) * 4 + c) * 64 + lane) * 16, 0, SC1);
    }
  };
  auto g2_wave7 = [&](bool rest) {
    bf16x8 W7[6][5];
    #pragma unroll
    for (int i = 0; i < 6; i++) {
      if (i == 3 && rest) { ST(2); bar(); }
      #pragma unroll
      for (int c = 0; c < 5; c++) W7[i][c] = ldWs(kidx7(i), c);
    }
    lds_put(l0 + L_RDY + 12, 1);
    if (rest) { wd_to_lds(0, 11); bar(); }
    poll(ws, O_FH + (rt * 32 + (lane < 27 ? lane : 0)) * FPS, tag);
    lds_put(l0 + L_FLAG, 1);
    __builtin_amdgcn_s_setprio(1);
    ST(5);
    bf16x8 H[3][4];
    #pragma unroll
    for (int i = 0; i < 2; i++)
      #pragma unroll
      for (int t = 0; t < 4; t++) H[i][t] = ldHs(kidx7(i), t);
    f32x4 acc[4][5];
    #pragma unroll
    for (int i = 0; i < 6; i++)
      #pragma unroll
      for (int c = 0; c < 5; c++)
        #pragma unroll
        for (int t = 0; t < 4; t++) {
          const int tt = (c & 1) ? 3 - t : t;
          if (i == 0) mf16z(acc[tt][c], W7[i][c], H[i % 3][tt]); else mf16(acc[tt][c], W7[i][c], H[i % 3][tt]);
          if (t == 3 && c < 4 && i + 2 < 6) H[(i + 2) % 3][c] = ldHs(kidx7(i + 2), c);
        }
    load_x();
    mf_pad();
    lds_wait4<2>(l0 + L_RDY, 1);
    put_acc(l0 + L_CONV, acc);
    ST(6);
  };

  if (gemm) {
    if (w >= 4) {
      const int j = w - 4, gu = j & 1;
      unsigned xo[8], wo[4];
      #pragma unroll
      for (int i = 0; i < 8; i++) xo[i] = (lane >> 3) * 1280 + (((lane & 7) ^ ((4 * i + (lane >> 4)) & 7)) * 16) + 9216 * i + 4096;
      #pragma unroll
      for (int i = 0; i < 4; i++) wo[i] = (lane >> 3) * 3456 + (((lane & 7) ^ (2 * (((lane >> 4) & 1) | ((i & 1) << 1)))) * 16) + 26624 * i;
      const char* xb = (const char*)x + (size_t)(panel * 256 + j * 64) * 1280;
      const char* wb = (const char*)(gu ? wu : wg) + (size_t)(32 * (j >> 1)) * 3456 + slice * 128;
      const unsigned lw = l0 + 32768 + gu * 8192 + (j >> 1) * 4096, lx = l0 + j * 8192 + 4096;
      auto issue = [&](unsigned so) {
        set_m0(lw + so);
        gl<0>(wo[0], wb); gl<1024>(wo[1], wb); gl<2048>(wo[2], wb); gl<3072>(wo[3], wb);
        set_m0(lx + so);
        gl<-4096>(xo[0], xb); gl<-3072>(xo[1], xb); gl<-2048>(xo[2], xb); gl<-1024>(xo[3], xb); gl<0>(xo[4], xb); gl<1024>(xo[5], xb); gl<2048>(xo[6], xb); gl<3072>(xo[7], xb);
        xb += 128; wb += 64 * 3456;
      };
      const unsigned FLA = l0 + L_S + 4608;
      auto flags_ok = [&](unsigned a, int n) {
        const u32x4 v = *(volatile LDS u32x4*)(size_t)(a + (lane < n ? lane : 0) * 16);
        return __all(v[0] == (unsigned)tag && v[2] == (unsigned)tag && v[1] == (unsigned)(tag >> 32) && v[3] == (unsigned)(tag >> 32));
      };
      issue(0); issue(2 * SLOT);
      GET_TAG(12);
      ST(8);
      bar();
      issue(SLOT);
      unsigned so = 0;
      #pragma unroll 1
      for (int s = 1; s < 8; s++) {
        wait_vm<12>();
        bar();
        if (s == 7 && j == 3 && lane < 20) glds1(FLA, O_FA + lane * 16, ws);
        issue(so);
        so = so == 0 ? 2 * SLOT : so - SLOT;
      }
      wait_vm<12>(); bar();
      const bool faok = flags_ok(FLA, 20);
      const int pos = 3 * gi + j;
      const bool hasc = j < 3 && pos < 640;
      const unsigned KB = l0 + L_CONV + j * CONVB, UB = KB + KXB;
      auto get_stats = [&]() { touch1(l0 + L_ST + j * 256, O_STAT + (panel * 256 + j * 64 + lane) * 4, ws); };
      get_stats();
      wait_vm<1>();
      if (faok && j < 3) wd_to_lds(11 + 3 * j, 13 + 3 * j);
      ST(1);
      bar();
      if (!faok) { poll(ws, O_FA + (lane < 40 ? lane : 0) * 8, tag); get_stats(); wait_vm<0>(); } else if (j < 3) wait_vm<10>(); else wait_vm<0>();
      if (j == 3) {
        g2_wave7(true);
      } else {
        if (hasc) {
          const char* ub = ws + O_XNB + pos * TP;
          set_m0(UB + 512);
          gl1<0>(lane * 16, ub); gl1<1024>(lane * 16, ub); gl1<2048>(lane * 16, ub); gl1<3072>(lane * 16, ub);
          *(LDS u32x2*)(size_t)(UB + lane * 8) = u32x2{0, 0};
        }
        ST(2);
        bar();
        wd_to_lds(faok ? 13 + 3 * j : 11 + 3 * j, 14 + 3 * j);
        const unsigned dvb = *(const GAS u16*)(dsk + pos2ch(hasc ? pos : 0));
        load_x();
        if (hasc) kexpand(*(LDS u32x4*)(size_t)(l0 + L_KC + j * 1536 + lane * 16), *(LDS u32x2*)(size_t)(l0 + L_KC + j * 1536 + 1024 + lane * 8), KB, lane);
        wait_vm<0>(); wait_lgkm0m();
        bar();
        ST(4);
        if (hasc) {
          __builtin_amdgcn_s_setprio(1);
          conv(KB, UB, bf_lo(dvb), rw, O_ST + pos2ch(pos) * TP, lane);
        }
        ST(5);
        lds_put(l0 + L_RDY + 4 * j, 1);
        wait_vm<0>();
        unsigned n = 0;
        if (lane == 0) n = __hip_atomic_fetch_add((LDS unsigned*)(size_t)(l0 + L_FLAG + 4), 1u, __ATOMIC_RELAXED, __HIP_MEMORY_SCOPE_WORKGROUP);
        if (lane == 0 && n == 2) __hip_atomic_store((GAS u64*)(ws + O_FS + gi * FPS), tag, RLX);
        ST(3);
      }
    } else {
      if (threadIdx.x < 16) *(volatile LDS unsigned*)(size_t)(l0 + L_FULL + 4 * lane) = 0;
      const bool hask = w < 3 && 3 * gi + w < 640;
      const KPar P = kpar_load(lar, aim, cre, cim, ldt, pos2ch(hask ? 3 * gi + w : 0), lane);
      unsigned wa[4], xa[2], wb[4], xb[2];
      #pragma unroll
      for (int u = 0; u < 4; u++) wa[u] = l0 + 32768 + (8 * g + q) * 128 + ((((u + w) & 3) ^ ((q >> 1) | ((g & 1) << 1))) * 32) + 8 * p;
      #pragma unroll
      for (int k = 0; k < 2; k++) xa[k] = l0 + (w * 64 + (lane & 15)) * 128 + (((4 * k + g) ^ ((lane >> 1) & 7)) * 16);
      f32x4 acc[8][4];
      f32x4 cacc[2];
      bf16x8 ones = __builtin_bit_cast(bf16x8, u32x4{0x3f803f80u, 0x3f803f80u, 0x3f803f80u, 0x3f803f80u});
      asm volatile("" : "+v"(ones));
      bf16x8 X[2][4]; s16x4 W[4][2];
      auto rdx = [&](int k, int t) { X[k][t] = *(LDS bf16x8*)((LDS char*)(size_t)xb[k] + t * 2048); };
      auto rdw = [&](int n, int e) { const int k = n >> 3, f = n & 7; W[n & 3][e] = __builtin_amdgcn_ds_read_tr16_b64_v4i16((LDS s16x4*)((LDS char*)(size_t)wb[f >> 1] + (f & 1) * 8192 + k * 4096 + e * 512)); };
      auto wf = [&](int n) { return __builtin_bit_cast(bf16x8, __builtin_shufflevector(W[n & 3][0], W[n & 3][1], 0, 1, 2, 3, 4, 5, 6, 7)); };
      auto mf = [&](int n, int t, bool z = false) { const int k = n >> 3, f = n & 7, tt = (f & 1) ? 3 - t : t;
        if (z) mf16z(acc[f][tt], wf(n), X[k][tt]); else mf16(acc[f][tt], wf(n), X[k][tt]); };
      auto base = [&](unsigned& d, unsigned a, unsigned so) { asm volatile("v_add_u32 %0, %1, %2" : "=v"(d) : "i"(so), "v"(a)); };
      auto body = [&](unsigned son, bool first) {
        #pragma unroll
        for (int n = 0; n < 13; n++) {
          const bool z = first && n < 8;
          mf(n, 0, z); rdw(n + 3, 0); mf(n, 1, z); rdw(n + 3, 1); mf(n, 2, z);
          if (n < 4) rdx(1, n);
          if (n == 4) base(xb[0], xa[0], son);
          if (n == 5) base(xb[1], xa[1], son);
          if (n == 7) base(wb[0], wa[0], son);
          if (n == 9) base(wb[1], wa[1], son);
          if (n == 11) base(wb[2], wa[2], son);
          mf(n, 3, z);
          if ((n & 6) == 0) { if (z) mf16z(cacc[n & 1], wf(n), ones); else mf16(cacc[n & 1], wf(n), ones); }
        }
        base(wb[3], wa[3], son);
      };
      #pragma unroll
      for (int u = 0; u < 4; u++) wb[u] = wa[u];
      xb[0] = xa[0]; xb[1] = xa[1];
      asm volatile("" : "+v"(wb[0]), "+v"(wb[1]), "+v"(wb[2]), "+v"(wb[3]), "+v"(xb[0]), "+v"(xb[1]));
      GET_TAG(0);
      ST(8);
      u32x4 o01 = {0, 0, 0, 0}; u32x2 o2 = {0, 0};
      if (hask) {
        kgen(P, l0 + SLOT + w * 8448, w < 2 ? l0 + SLOT + (3 + w) * 8448 : l0 + L_S, o01, o2, lane);
      } else if (w == 3) {
        if (PFD) __builtin_amdgcn_s_sleep(PFD);
        const int s0 = ((xcd & 3) * 27) >> 2, ns = (((xcd & 3) * 27 + 26) >> 2) - s0 + 1;
        #pragma unroll
        for (int i = 0; i < 3; i++) {
          const int n = (i * 27 + r) * 51 + min(lane, 50), k = min(128 + n / ns, 639), c = n % ns;
          if (lane < 51) touch(l0 + L_DUMP, (const char*)((xcd >> 2) ? wu : wg) + (size_t)k * 3456 + (s0 + c) * 128);
        }
      }
      ST(9);
      wait_lgkm0m();
      bar();
      ST(1);
      #pragma unroll
      for (int t = 0; t < 4; t++) rdx(0, t);
      #pragma unroll
      for (int n = 0; n < 3; n++) { rdw(n, 0); rdw(n, 1); }
      body(2 * SLOT, true);
      #pragma unroll
      for (int s = 1; s < 10; s++) {
        sb(); wait_lgkm0(); bar();
        mf(13, 0); rdw(0, 0); mf(13, 1); rdw(0, 1); mf(13, 2); rdx(0, 0); mf(13, 3); rdx(0, 1);
        mf(14, 0); rdw(1, 0); mf(14, 1); rdw(1, 1); mf(14, 2); rdx(0, 2); mf(14, 3); rdx(0, 3);
        mf(15, 0); rdw(2, 0); mf(15, 1); rdw(2, 1); mf(15, 2); mf(15, 3);
        body(((s + 1) % 3 == 0 ? 0 : (s + 1) % 3 == 1 ? 2 : 1) * SLOT, false);
      }
      sb();
      asm volatile("" : "+v"(cacc[0]), "+v"(cacc[1]));
      if ((lane & 15) == 0) { *(LDS f32x4*)(size_t)(l0 + L_CS + w * 128 + g * 16) = cacc[0]; *(LDS f32x4*)(size_t)(l0 + L_CS + w * 128 + 64 + g * 16) = cacc[1]; }
      if (w < 3) { *(LDS u32x4*)(size_t)(l0 + L_KC + w * 1536 + lane * 16) = o01; *(LDS u32x2*)(size_t)(l0 + L_KC + w * 1536 + 1024 + lane * 8) = o2; }
      wait_lgkm0m();
      bar();
      #pragma unroll
      for (int n = 13; n < 16; n++) { mf(n, 0); mf(n, 1); mf(n, 2); mf(n, 3); }
      __builtin_amdgcn_s_setprio(2);
      ST(2);
      float nm[4], k0[4], k1[4], sq[4];
      #pragma unroll
      for (int t = 0; t < 4; t++) {
        unsigned st = *(LDS unsigned*)(size_t)(l0 + L_ST + (w * 64 + t * 16 + (lane & 15)) * 4);
        float den = bf_hi(st), ri = __builtin_amdgcn_rcpf(den);
        nm[t] = -bf_lo(st); k0[t] = -2.3022082f * ri; k1[t] = -2.3022082f * 0.044715f * ri * ri * ri; sq[t] = den * den;
      }
      #pragma unroll
      for (int f = 0; f < 8; f++) asm volatile("s_nop 0" : "+v"(acc[f][0]), "+v"(acc[f][1]), "+v"(acc[f][2]), "+v"(acc[f][3]));
      #pragma unroll
      for (int u = 0; u < 4; u++) {
        const int ug = (u + w) & 3;
        const f32x4 cg = *(LDS f32x4*)(size_t)(l0 + L_CS + ug * 128 + g * 16), cu = *(LDS f32x4*)(size_t)(l0 + L_CS + ug * 128 + 64 + g * 16);
        unsigned o[4][2];
        #pragma unroll
        for (int t = 0; t < 4; t++)
          #pragma unroll
          for (int e = 0; e < 2; e++) {
            f32x2 gg = f32x2{cg[2 * e], cg[2 * e + 1]} * nm[t] + f32x2{acc[2 * u][t][2 * e], acc[2 * u][t][2 * e + 1]};
            f32x2 uu = f32x2{cu[2 * e], cu[2 * e + 1]} * nm[t] + f32x2{acc[2 * u + 1][t][2 * e], acc[2 * u + 1][t][2 * e + 1]};
            f32x2 a = gg * (gg * gg * k1[t] + k0[t]);
            f32x2 ee = {__builtin_amdgcn_exp2f(a[0]), __builtin_amdgcn_exp2f(a[1])};
            ee = ee * sq[t] + sq[t];
            f32x2 hv = gg * uu * f32x2{__builtin_amdgcn_rcpf(ee[0]), __builtin_amdgcn_rcpf(ee[1])};
            o[t][e] = pk2(hv[0], hv[1]);
          }
        #pragma unroll
        for (int k = 0; k < 2; k++) {
          auto r0 = __builtin_amdgcn_permlane16_swap(o[2 * k][0], o[2 * k + 1][0], false, false), r1 = __builtin_amdgcn_permlane16_swap(o[2 * k][1], o[2 * k + 1][1], false, false);
          __builtin_amdgcn_raw_buffer_store_b128(u32x4{r0[0], r1[0], r0[1], r1[1]}, rw, O_HT + (((panel * 8 + w * 2 + k) * 108 + slice * 4 + ug) * 64 + lane) * 16, 0, SC1);
        }
      }
      bar();
      __builtin_amdgcn_s_setprio(0);
      ST(3);
      publish(ws, O_FH + ((panel * 4 + w) * 32 + slice) * FPS, tag, lane);
      ST(4);
    }
  } else {
    const int ai = xcd * 5 + r - 27, job = w * 40 + ai, job2 = 320 + (7 - w) * 40 + 39 - ai;
    const bool hasln = r < 31;
    const int tg = xcd * 4 + r - 27;
    const unsigned R = l0 + 81920 + w * 8192;
    unsigned xv[8][5];
    if (hasln) {
      const GAS unsigned* xp = (const GAS unsigned*)x + (tg * 8 + w) * 2560 + lane;
      #pragma unroll
      for (int i = 0; i < 8; i++)
        #pragma unroll
        for (int jj = 0; jj < 5; jj++) xv[i][jj] = xp[i * 320 + jj * 64];
    }
    rt_load(job, R);
    if (job2 < 540) rt_load(job2, R + 4096);
    if (hasln) layernorm(xv, tg * 8 + w, l0, rw, lane);
    ST(9);
    GET_TAG(0);
    rt_store(job, R);
    if (job2 < 540) rt_store(job2, R + 4096);
    if (w == 7) lds_put(l0 + L_FLAG, 0);
    wait_lgkm0m(); bar();
    if (hasln) {
      #pragma unroll
      for (int i = 0; i < 10; i++) {
        const int ps = (w * 10 + i) * 8 + (lane >> 3);
        __builtin_amdgcn_raw_buffer_store_b128(*(LDS u32x4*)(size_t)(l0 + ps * 128 + (((lane ^ ps) & 7) << 4)), rw, O_XNB + ps * TP + tg * 128 + (lane & 7) * 16, 0, SC1);
      }
    }
    ST(10);
    wait_vm<0>(); bar();
    if (w == 0 && lane == 0) __hip_atomic_store((GAS u64*)(ws + O_FA + ai * 8), tag, RLX);
    ST(2);
    if (w == 7) { __builtin_amdgcn_s_sleep(AUXS); poll(ws, O_FA + (lane < 40 ? lane : 0) * 8, tag); }
    bar();
    ST(11);
    if (w >= 4 && w < 7) { wd_to_lds(7 * (w - 4), w == 6 ? 20 : 7 * (w - 3)); wait_vm<0>(); lds_put(l0 + L_RDY + 4 * (w - 4), 1); wait_lgkm0m(); }
    bar();
    if (w == 7) g2_wave7(false);
  }

  if (w == 7) {
  } else if (w >= 4) {
    if (!gemm) load_x();
    if (w == 4) {
      auto prod = [&](int c) { return O_FS + (((c & ~127) + (c & 1) * 64 + ((c & 127) >> 1)) / 3) * FPS; };
      poll(ws, prod(cs * 80 + lane), prod(cs * 80 + 64 + (lane & 15)), tag);
      #pragma unroll
      for (int i = 0; i < 10; i++) glds1(l0 + L_S + i * 1024, (lane >> 3) * TP + (((lane & 7) ^ (2 * ((lane >> 4) & 3))) * 16), ws + O_ST + (size_t)(cs * 80 + i * 8) * TP + rt * 128);
    } else if (w == 5 && b == 255) {
      #pragma unroll 1
      for (int i = 0; i < 4; i++) { int t = i * 64 + lane; poll(ws, t < 216 ? O_FH + ((t / 27) * 128 + t % 27) * FPS : O_FA + (t - 216) * 8, tag); }
      if (lane == 0) __hip_atomic_store((GAS u64*)(ws + O_E), tag, RLX);
    }
    wait_vm<0>();
    ST(6);
  } else {
    int ks[12];
    #pragma unroll
    for (int i = 0; i < 12; i++) ks[i] = kidx(w, i);
    bf16x8 W[4][5], H[4][4];
    #pragma unroll
    for (int i = 0; i < 4; i++)
      #pragma unroll
      for (int c = 0; c < 5; c++) W[i][c] = ldWs(ks[i], c);
    sb();
    lds_wait(l0 + L_FLAG);
    ST(5);
    constexpr int ord[12] = {0, 4, 1, 5, 2, 6, 3, 7, 9, 8, 10, 11};
    constexpr int set[12] = {0, 0, 1, 1, 2, 2, 3, 3, 0, 0, 1, 2};
    #pragma unroll
    for (int i = 0; i < 3; i++)
      #pragma unroll
      for (int t = 0; t < 4; t++) H[i][t] = ldHs(ks[ord[i]], t);
    f32x4 acc[4][5];
    const unsigned wl = l0 + lane * 16 + (w < 2 ? w * 25600 : 98304 + (w * 5 - 11) * 5120), wl0 = w == 2 ? l0 + lane * 16 + 51200 : wl;
    #pragma unroll
    for (int i = 0; i < 12; i++) {
      const bool inl = ord[i] >= 4 && ord[i] < 9, nxl = i < 11 && ord[i + 1] >= 4 && ord[i + 1] < 9;
      #pragma unroll
      for (int c = 0; c < 5; c++) {
        #pragma unroll
        for (int t = 0; t < 4; t++) {
          const int tt = (c & 1) ? 3 - t : t;
          if (i == 0) mf16z(acc[tt][c], W[set[i]][c], H[i & 3][tt]); else mf16(acc[tt][c], W[set[i]][c], H[i & 3][tt]);
          if (t == 0 && c < 4 && i + 3 < 12) H[(i + 3) & 3][c] = ldHs(ks[ord[i + 3]], c);
        }
        if (nxl) W[set[i]][c] = *(LDS bf16x8*)((LDS char*)(size_t)(ord[i + 1] == 4 ? wl0 : wl) + (ord[i + 1] - 4) * 5120 + c * 1024);
        else if (inl && ord[i] < 7) W[set[i]][c] = ldWs(ks[ord[i] + 5], c);
      }
      if (i == XLD) load_x();
    }
    mf_pad();
    ST(6);
    put_acc(w == 2 ? l0 + wslot(11) : l0 + (w < 2 ? w * 25600 : wslot(15)), acc);
  }
  wait_lgkm0m();
  bar();
  ST(7);
  {
    const rsrc_t ro = __builtin_amdgcn_make_buffer_rsrc(out, 0, L * D * 2, 0x00020000);
    #pragma unroll
    for (int c = 0; c < 3; c++) {
      if (c == 2 && w >= 4) break;
      LDS char* a = (LDS char*)(size_t)(l0 + (tt_ * 5 + c0 + c) * 1024 + lane * 16);
      const f32x4 o = ((*(LDS f32x4*)a + *(LDS f32x4*)(a + 25600)) + (*(LDS f32x4*)(a + L_CONV) + (*(LDS f32x4*)(a + wslot(11)) + *(LDS f32x4*)(a + wslot(15)))));
      const int row = (c0 + c) * 16 + 4 * g + q;
      const u32x2 sv = __builtin_bit_cast(u32x2, trd(l0 + L_S + row * 128 + ((tt_ ^ ((row >> 1) & 3)) * 32) + 8 * p));
      u32x2 y;
      #pragma unroll
      for (int e = 0; e < 2; e++) {
        const f32x2 oo = {o[2 * e], o[2 * e + 1]}, a2 = oo * -1.4426950408889634f;
        const f32x2 d = f32x2{__builtin_amdgcn_exp2f(a2[0]), __builtin_amdgcn_exp2f(a2[1])} + 1.f;
        const f32x2 v = oo * f32x2{__builtin_amdgcn_rcpf(d[0]), __builtin_amdgcn_rcpf(d[1])} * f32x2{bf_lo(sv[e]), bf_hi(sv[e])} + f32x2{bf_lo(xr[c][e]), bf_hi(xr[c][e])};
        y[e] = pk2(v[0], v[1]);
      }
      __builtin_amdgcn_raw_buffer_store_b64(y, ro, ((rt * 64 + tt_ * 16 + (lane & 15)) * D + cs * 80 + (c0 + c) * 16 + g * 4) * 2, 0, OUTAUX);
    }
  }
  if (w != 7) ST(15);
  ST_DUMP();
}

extern "C" void kernel_launch(void* const* d_in, const int* in_sizes, int n_in, void* d_out, int out_size, void* d_ws, size_t ws_size, hipStream_t stream) {
  (void)hipFuncSetAttribute((const void*)fused_kernel, hipFuncAttributeMaxDynamicSharedMemorySize, LDS_BYTES);
  fused_kernel<<<256, 512, LDS_BYTES, stream>>>((const u16*)d_in[0], (const u16*)d_in[1], (const u16*)d_in[2], (const u16*)d_in[3], (const u16*)d_in[4], (const u16*)d_in[5],
      (const u16*)d_in[6], (const u16*)d_in[8], (const u16*)d_in[9], (const u16*)d_in[10], (u16*)d_out, (char*)d_ws);
}
```
